```python
import torch

def _softmax(x, axis=-1):
    return torch.softmax(x, dim=axis)
def _gelu(x):
    return torch.nn.functional.gelu(x, approximate="tanh")
def _silu(x):
    return torch.nn.functional.silu(x)
def _sigmoid(x):
    return torch.sigmoid(x)
def _layernorm(x, axis=-1, eps=1e-5):
    m = x.mean(dim=axis, keepdim=True)
    v = x.var(dim=axis, keepdim=True, unbiased=False)
    return (x - m) / torch.sqrt(v + eps)
def _scatter_add(tgt, idx, src, axis=0):
    return tgt.clone().index_add_(axis, idx, src)
def _segment_sum(src, seg, n):
    out = torch.zeros((n,) + src.shape[1:], dtype=src.dtype)
    return out.index_add_(0, seg, src)
def _pad(x, pw):
    flat = tuple(v for pair in reversed(pw) for v in pair)
    return torch.nn.functional.pad(x, flat)

kernel_name = 'parallel_sum__sliding_block_cached__dense_activation_gelu__decode'

def setup_inputs():

    _w = (lambda a: (lambda t: t.to(torch.bfloat16) if t.is_floating_point() else t)(torch.as_tensor(a)))
    def _A_setup():
        B, Sq, D = 1, 1, 640
        Hq, Hkv, Sk, Dh = 32, 32, 8192, 128
        assert Hq % Hkv == 0, (Hq, Hkv)
        return {
            "x": _w((lambda *s: torch.randn(*s, dtype=torch.bfloat16) * 0.05)(B, Sq, D)),
            "k_cache": _w((lambda *s: torch.randn(*s, dtype=torch.bfloat16) * 3.2)(B, Hkv, Sk, Dh)),
            "v_cache": _w((lambda *s: torch.randn(*s, dtype=torch.bfloat16) * 0.8)(B, Hkv, Sk, Dh)),
            "wq": _w((lambda *s: torch.randn(*s, dtype=torch.bfloat16) * 3.2)(D, Hq * Dh) * (D ** -0.5)),
            "wo": _w((lambda *s: torch.randn(*s, dtype=torch.bfloat16) * 0.8)(Hq * Dh, D) * ((Hq * Dh) ** -0.5)),
        }
    _A_in = _A_setup()

    def _B_setup():
        B, S, D, F = 1, 1, 640, 1728
        return {
            "x": _w((lambda *s: torch.randn(*s, dtype=torch.bfloat16) * 0.05)(B, S, D)),
            "w1": _w((lambda *s: torch.randn(*s, dtype=torch.bfloat16) * 0.8)(D, F) * (D ** -0.5)),
            "b1": _w((lambda *s: torch.randn(*s, dtype=torch.bfloat16) * 0.8)(F) * 0.01),
            "w2": _w((lambda *s: torch.randn(*s, dtype=torch.bfloat16) * 0.8)(F, D) * (F ** -0.5)),
            "b2": _w((lambda *s: torch.randn(*s, dtype=torch.bfloat16) * 0.8)(D) * 0.01),
        }
    _B_in = _B_setup()

    return {
        "A_x": _A_in["x"],
        "A_k_cache": _A_in["k_cache"],
        "A_v_cache": _A_in["v_cache"],
        "A_wq": _A_in["wq"],
        "A_wo": _A_in["wo"],
        "B_x": _B_in["x"],
        "B_w1": _B_in["w1"],
        "B_b1": _B_in["b1"],
        "B_w2": _B_in["w2"],
        "B_b2": _B_in["b2"],
    }

def reference(A_x, A_k_cache, A_v_cache, A_wq, A_wo, B_x, B_w1, B_b1, B_w2, B_b2):
    def _A_ref(x, k_cache, v_cache, wq, wo):
        B, Sq, D = x.shape
        Hq, Dh = 32, 128
        Hkv, Sk = k_cache.shape[1], k_cache.shape[2]
        W = min(4096, max(256, Sk // 4))
        rep = Hq // Hkv
        q = (lambda x, *d: x.permute(*d))((x @ wq).reshape(B, Sq, Hq, Dh), 0, 2, 1, 3)
        k = (lambda x, n, axis: torch.repeat_interleave(x, n, dim=axis))(k_cache[:, :, Sk - W:, :], rep, axis=1)
        v = (lambda x, n, axis: torch.repeat_interleave(x, n, dim=axis))(v_cache[:, :, Sk - W:, :], rep, axis=1)
        scores = torch.einsum("bhqd,bhkd->bhqk", q, k) * (Dh ** -0.5)
        p = _softmax(scores, axis=-1)
        o = torch.einsum("bhqk,bhkd->bhqd", p, v)
        return (lambda x, *d: x.permute(*d))(o, 0, 2, 1, 3).reshape(B, Sq, Hq * Dh) @ wo

    def _B_ref(x, w1, b1, w2, b2):
        h = torch.einsum("bsd,df->bsf", x, w1) + b1
        h = _gelu(h)
        return torch.einsum("bsf,fd->bsd", h, w2) + b2

    x = A_x
    xn = _layernorm(x, axis=-1)
    return x + _A_ref(xn, A_k_cache, A_v_cache, A_wq, A_wo) + _B_ref(xn, B_w1, B_b1, B_w2, B_b2)
```

```cpp
#include <hip/hip_runtime.h>
#include <hip/hip_bf16.h>
#include <stdint.h>

#define MIMIC 1

typedef unsigned u32;
typedef unsigned long long u64;
typedef u32 u32x4 __attribute__((ext_vector_type(4)));
typedef u32 u32x2 __attribute__((ext_vector_type(2)));
typedef float f32x4 __attribute__((ext_vector_type(4)));
typedef float f32x2 __attribute__((ext_vector_type(2)));
typedef __bf16 bf16x8 __attribute__((ext_vector_type(8)));
typedef __bf16 bf16x2 __attribute__((ext_vector_type(2)));
typedef __amdgpu_buffer_rsrc_t rsrc_t;

constexpr int D = 640, NH = 32, DH = 128, SK = 8192, WIN = 2048, FF = 1728;
constexpr int AUX_NT = 2, AUX_SC1 = 16;
constexpr u32 OOB = 0x80000000u;
constexpr u32 WS_QG = 4096;
constexpr u32 WS_OG = WS_QG + 256 * 1024;
constexpr u32 WS_PG = WS_OG + 256 * 1024;
constexpr u32 WS_MG = WS_PG + 256 * 512;
constexpr u32 WS_YG = WS_MG + 256 * 128;
constexpr u32 WS_END = WS_YG + 40 * 4096;
constexpr int SPIN_CAP = 1 << 18;
constexpr int L_X = 0;
constexpr int L_QP = L_X + 8 * 1280;
constexpr int L_PP = L_QP + 4096;
constexpr int L_Q = L_PP + 4096;
constexpr int L_OP = L_Q + 256;
constexpr int L_ML = L_OP + 4096;
constexpr int L_YP = L_ML + 64;
constexpr int L_A = L_YP + 8 * 320;
constexpr int LDS_BYTES = 84 * 1024;

#define SB() __builtin_amdgcn_sched_barrier(0)
#define MEMFENCE() asm volatile("" ::: "memory")

__device__ __forceinline__ rsrc_t mk(const void* p, u32 n) {
  return __builtin_amdgcn_make_buffer_rsrc((void*)p, 0, n, 0x00020000);
}
__device__ __forceinline__ u32x4 ld16(rsrc_t r, u32 v, u32 s, const int aux_nt) {
  return aux_nt ? __builtin_amdgcn_raw_buffer_load_b128(r, v, s, AUX_NT) : __builtin_amdgcn_raw_buffer_load_b128(r, v, s, 0);
}
__device__ __forceinline__ u32x4 ldg(rsrc_t r, u32 v) { return __builtin_amdgcn_raw_buffer_load_b128(r, v, 0, AUX_SC1); }
__device__ __forceinline__ void stg(rsrc_t r, u32 v, float a, float b, u32 ta, u32 tb) {
  u32x4 g = {__float_as_uint(a), ta, __float_as_uint(b), tb};
  __builtin_amdgcn_raw_buffer_store_b128(g, r, v, 0, AUX_SC1);
}
__device__ __forceinline__ bool wave_ok(u32x4 g, u32 ta, u32 tb) {
  return __builtin_amdgcn_ballot_w64(g.y != ta || g.w != tb) == 0;
}
template <int CTRL> __device__ __forceinline__ float dpp(float x) {
  return __builtin_bit_cast(float, __builtin_amdgcn_update_dpp(0, __builtin_bit_cast(int, x), CTRL, 0xf, 0xf, true));
}
__device__ __forceinline__ float rsum16(float x) {
  x += dpp<0x128>(x); x += dpp<0x124>(x); x += dpp<0x122>(x); x += dpp<0x121>(x); return x;
}
__device__ __forceinline__ float xsum16(float x) {
  auto r = __builtin_amdgcn_permlane16_swap(__float_as_uint(x), __float_as_uint(x), false, false);
  return __uint_as_float(r[0]) + __uint_as_float(r[1]);
}
__device__ __forceinline__ float xsum32(float x) {
  auto r = __builtin_amdgcn_permlane32_swap(__float_as_uint(x), __float_as_uint(x), false, false);
  return __uint_as_float(r[0]) + __uint_as_float(r[1]);
}
__device__ __forceinline__ float xmax16(float x) {
  auto r = __builtin_amdgcn_permlane16_swap(__float_as_uint(x), __float_as_uint(x), false, false);
  return fmaxf(__uint_as_float(r[0]), __uint_as_float(r[1]));
}
__device__ __forceinline__ float xmax32(float x) {
  auto r = __builtin_amdgcn_permlane32_swap(__float_as_uint(x), __float_as_uint(x), false, false);
  return fmaxf(__uint_as_float(r[0]), __uint_as_float(r[1]));
}
__device__ __forceinline__ float ssum8(float x) { x += dpp<0x128>(x); return xsum32(xsum16(x)); }
__device__ __forceinline__ float smax8(float x) { x = fmaxf(x, dpp<0x128>(x)); return xmax32(xmax16(x)); }
__device__ __forceinline__ u32 bfbits(float x) {
  return (u32)__builtin_bit_cast(unsigned short, (__bf16)x);
}
__device__ __forceinline__ float bfr(float x) { return __uint_as_float(bfbits(x) << 16); }
__device__ __forceinline__ float lo16(u32 x) { return __uint_as_float(x << 16); }
__device__ __forceinline__ float hi16(u32 x) { return __uint_as_float(x & 0xffff0000u); }
__device__ __forceinline__ float dot2(u32 a, u32 b, float c) {
  return __builtin_amdgcn_fdot2_f32_bf16(__builtin_bit_cast(bf16x2, a), __builtin_bit_cast(bf16x2, b), c, false);
}
__device__ __forceinline__ f32x4 mma(u32x4 a, u32 bits2, const u32 (&mk4)[4], f32x4 acc) {
  u32x4 b = {bits2 & mk4[0], bits2 & mk4[1], bits2 & mk4[2], bits2 & mk4[3]};
  return __builtin_amdgcn_mfma_f32_16x16x32_bf16(__builtin_bit_cast(bf16x8, a), __builtin_bit_cast(bf16x8, b), acc, 0, 0, 0);
}
__device__ __forceinline__ u32 hilo(float v, bool low) {
  float h = bfr(v);
  u32 b = bfbits(low ? v - h : v);
  return b | (b << 16);
}

#define STAMP(i)

__global__ void __launch_bounds__(512, 2)
fused_decode_kernel(const unsigned short* __restrict__ x, const unsigned short* __restrict__ kc,
                    const unsigned short* __restrict__ vc, const unsigned short* __restrict__ wq,
                    const unsigned short* __restrict__ wo, const unsigned short* __restrict__ w1,
                    const unsigned short* __restrict__ b1, const unsigned short* __restrict__ w2,
                    const unsigned short* __restrict__ b2, unsigned short* __restrict__ out, char* ws) {
  extern __shared__ __attribute__((aligned(16))) char lds[];
  const int lane = threadIdx.x & 63;
  const int w = __builtin_amdgcn_readfirstlane(threadIdx.x >> 6);
  const int blk = blockIdx.x;
  const int h = (blk >> 6) * 8 + (blk & 7), s = (blk >> 3) & 7;
  const int n = lane & 15, g = lane >> 4;
  const bool mlp = h < FF / 64;
  STAMP(0);

  u64 ep;
  asm volatile("s_load_dwordx2 %0, %1, 0x0" : "=s"(ep) : "s"(ws) : "memory");

  const rsrc_t rx = mk(x, D * 2);
  const rsrc_t rwq = mk(wq + (size_t)(80 * s + 10 * w) * 4096 + h * 128, 10 * 8192);
  const rsrc_t rw1 = mk(w1 + (size_t)(80 * s + 10 * w) * FF + (mlp ? h : 0) * 64, mlp ? 9 * FF * 2 + 128 : 0);
  const size_t kvo = ((size_t)h * SK + (SK - WIN) + 256 * s + 4 * w) * DH;
  const rsrc_t rk = mk(kc + kvo, 65536 - 1024 * w), rv = mk(vc + kvo, 65536 - 1024 * w);
  const rsrc_t rwo = mk(wo + (size_t)(h * 128 + 16 * w) * D + 80 * s, 15 * 1280 + 160);
  const rsrc_t rw2 = mk(w2 + (size_t)((mlp ? h : 0) * 64 + 8 * w) * D + 80 * s, mlp ? 7 * 1280 + 160 : 0);
  const rsrc_t rws = mk(ws, WS_END);

  u32x4 xa = ld16(rx, lane * 16, 0, 0);
  u32x4 xb = ld16(rx, 1024 + lane * 16, 0, 0);
  SB();
  u32x4 aq[3], a1[2];
  aq[0] = ld16(rwq, g * 8192 + n * 16, 0, 1);
  aq[1] = ld16(rwq, (4 + g) * 8192 + n * 16, 0, 1);
  aq[2] = ld16(rwq, g < 2 ? (8 + g) * 8192 + n * 16 : OOB, 0, 1);
  a1[0] = ld16(rw1, (lane >> 3) * (FF * 2) + (lane & 7) * 16, 0, 1);
  a1[1] = ld16(rw1, lane < 16 ? (8 + (lane >> 3)) * (FF * 2) + (lane & 7) * 16 : OOB, 0, 1);
  SB();
  u32x4 kr[8], vr[8], wr[6];
#pragma unroll
  for (int i = 0; i < 4; ++i) kr[i] = ld16(rk, lane * 16, i * 8192, 1);
  SB();
  const u32 b1v = __builtin_amdgcn_raw_buffer_load_b32(mk(b1, mlp ? FF * 2 : 0), (h * 64 + 8 * w + 2 * (lane & 3)) * 2, 0, 0);
  const u32 fxv = __builtin_amdgcn_raw_buffer_load_b32(rx, (blk * 16 + 2 * (lane & 7)) * 2, 0, 0);
  const u32 fbv = __builtin_amdgcn_raw_buffer_load_b32(mk(b2, D * 2), (blk * 16 + 2 * (lane & 7)) * 2, 0, 0);
  SB();

  u32 mk4[4];
#pragma unroll
  for (int j = 0; j < 4; ++j) mk4[j] = (((n & 7) >> 1) == j) ? ((n & 1) ? 0xffff0000u : 0x0000ffffu) : 0u;
  const bool low = n >= 8;

  char* lx = lds + L_X + w * 1280;
  *(u32x4*)(lx + lane * 16) = xa;
  if (lane < 16) *(u32x4*)(lx + 1024 + lane * 16) = xb;
  float sm = 0.f;
#pragma unroll
  for (int j = 0; j < 4; ++j) sm += (lo16(xa[j]) + hi16(xa[j])) + (lo16(xb[j]) + hi16(xb[j]));
  const float mean = xsum32(xsum16(rsum16(sm))) * (1.0f / D);
  float sq = 0.f, sq2 = 0.f;
#pragma unroll
  for (int j = 0; j < 4; ++j) {
    float u0 = lo16(xa[j]) - mean, u1 = hi16(xa[j]) - mean, u2 = lo16(xb[j]) - mean, u3 = hi16(xb[j]) - mean;
    sq += u0 * u0 + u1 * u1; sq2 += u2 * u2 + u3 * u3;
  }
  sq += lane < 16 ? sq2 : 0.f;
  const float var = xsum32(xsum16(rsum16(sq))) * (1.0f / D);
  const float mu = bfr(mean), den = bfr(sqrtf(bfr(bfr(var) + 1e-5f)));
#define XN(r) bfr(bfr(lo16(*(const unsigned short*)(lx + 2 * (r))) - mu) / den)
  const int r0 = 80 * s + 10 * w;
  const float xq0 = XN(r0 + g), xq1 = XN(r0 + 4 + g), xq2 = g < 2 ? XN(r0 + 8 + (g & 1)) : 0.f;
  const float xp0 = XN(r0 + 2 * g + (n >> 3)), xp1 = g == 0 ? XN(r0 + 8 + (n >> 3)) : 0.f;
  f32x4 accq = {0.f, 0.f, 0.f, 0.f}, accp = {0.f, 0.f, 0.f, 0.f};
  accq = mma(aq[0], hilo(xq0, low), mk4, accq);
  accq = mma(aq[1], hilo(xq1, low), mk4, accq);
  accq = mma(aq[2], hilo(xq2, low), mk4, accq);
  accp = mma(a1[0], hilo(xp0, false), mk4, accp);
  accp = mma(a1[1], hilo(xp1, false), mk4, accp);
  STAMP(1);
  {
    float* qp = (float*)(lds + L_QP) + w * 128;
    float* pp = (float*)(lds + L_PP) + (2 * w + (g >> 1)) * 64;
#pragma unroll
    for (int j = 0; j < 4; ++j) {
      float v = accq[j] + dpp<0x128>(accq[j]);
      if (n < 8) qp[8 * (4 * g + j) + n] = v;
      if ((g >> 1) == (n >> 3)) pp[8 * (4 * (g & 1) + j) + (n & 7)] = accp[j];
    }
  }
  asm volatile("s_waitcnt lgkmcnt(0)" : "+s"(ep) :: "memory");
  u64 z = (ep + 1) * 0x9E3779B97F4A7C15ull;
  z = (z ^ (z >> 30)) * 0xBF58476D1CE4E5B9ull; z = (z ^ (z >> 27)) * 0x94D049BB133111EBull; z ^= z >> 31;
  const u32 ta = (u32)z, tb = (u32)(z >> 32);
  __builtin_amdgcn_s_barrier();
  MEMFENCE();
  const u32 hs = h * 8 + s;
  if (w == 0) {
    f32x2 a = {0.f, 0.f};
#pragma unroll
    for (int i = 0; i < 8; ++i) a += *(const f32x2*)(lds + L_QP + i * 512 + lane * 8);
    stg(rws, WS_QG + hs * 1024 + lane * 16, a.x, a.y, ta, tb);
  } else if (w == 1) {
    f32x2 a = {0.f, 0.f};
#pragma unroll
    for (int i = 0; i < 8; ++i) a += *(const f32x2*)(lds + L_PP + ((lane >> 5) * 8 + i) * 256 + (lane & 31) * 8);
    a.x = xsum32(a.x); a.y = xsum32(a.y);
    stg(rws, lane < 32 ? WS_PG + hs * 512 + lane * 16 : OOB, a.x, a.y, ta, tb);
  }
  MEMFENCE(); SB();
  STAMP(2);
#pragma unroll
  for (int i = 4; i < 8; ++i) kr[i] = ld16(rk, lane * 16, i * 8192, 1);
  SB();
  const u32 qoff = WS_QG + (h * 8 + (lane >> 3)) * 1024 + (w * 8 + (lane & 7)) * 16;
  u32x4 qg = ldg(rws, qoff);
  SB();
#pragma unroll
  for (int i = 0; i < 4; ++i) vr[i] = ld16(rv, lane * 16, i * 8192, 1);
  SB();
  if (!wave_ok(qg, ta, tb)) {
    for (int it = 0; it < SPIN_CAP; ++it) {
      MEMFENCE();
      qg = ldg(rws, qoff);
      if (wave_ok(qg, ta, tb)) break;
    }
  }
  MEMFENCE();
  STAMP(3);
  {
    float q0 = ssum8(__uint_as_float(qg.x)), q1 = ssum8(__uint_as_float(qg.z));
    if (lane < 8) *(u32*)(lds + L_Q + (w * 8 + lane) * 4) = bfbits(q0) | (bfbits(q1) << 16);
  }
  asm volatile("s_waitcnt lgkmcnt(0)" ::: "memory");
  __builtin_amdgcn_s_barrier();
  MEMFENCE();
  const u32x4 q = *(const u32x4*)(lds + L_Q + n * 16);
  float sc[8];
#pragma unroll
  for (int i = 0; i < 8; ++i) {
    float d = dot2(kr[i].x, q.x, 0.f);
    d = dot2(kr[i].y, q.y, d); d = dot2(kr[i].z, q.z, d); d = dot2(kr[i].w, q.w, d);
    d = rsum16(d);
    sc[i] = bfr(bfr(d) * 0.08838834764831845f);
    if (i & 1) { SB(); vr[4 + (i >> 1)] = ld16(rv, lane * 16, (4 + (i >> 1)) * 8192, 1); SB(); }
  }
  float m = fmaxf(fmaxf(fmaxf(sc[0], sc[1]), fmaxf(sc[2], sc[3])), fmaxf(fmaxf(sc[4], sc[5]), fmaxf(sc[6], sc[7])));
  m = xmax32(xmax16(m));
  float l = 0.f;
  u32 pb[8];
#pragma unroll
  for (int i = 0; i < 8; ++i) {
    float p = __expf(sc[i] - m);
    l += p;
    pb[i] = hilo(p, low);
  }
  l = xsum32(xsum16(l));
  STAMP(4);
  const u32 woff = n < 10 ? g * 1280 + n * 16 : OOB;
  f32x4 acco = {0.f, 0.f, 0.f, 0.f};
#pragma unroll
  for (int i = 0; i < 8; ++i) {
    acco = mma(vr[i], pb[i], mk4, acco);
    SB();
    if (i < 4) wr[i] = ld16(rwo, woff + i * 5120, 0, 1);
    else if (i < 6) wr[i] = ld16(rw2, woff + (i - 4) * 5120, 0, 1);
    SB();
  }
  {
    float* op = (float*)(lds + L_OP) + w * 128;
#pragma unroll
    for (int j = 0; j < 4; ++j) {
      float v = acco[j] + dpp<0x128>(acco[j]);
      if (n < 8) op[8 * (4 * g + j) + n] = v;
    }
    if (lane == 0) *(f32x2*)(lds + L_ML + w * 8) = f32x2{m, l};
  }
  asm volatile("s_waitcnt lgkmcnt(0)" ::: "memory");
  STAMP(5);
  __builtin_amdgcn_s_barrier();
  MEMFENCE();
  if (w == 0) {
    f32x2 ml = *(const f32x2*)(lds + L_ML + (lane >> 3) * 8);
    const float M = smax8(ml.x);
    const float e = __expf(ml.x - M);
    const float L = ssum8(ml.y * e);
    f32x2 a = {0.f, 0.f};
#pragma unroll
    for (int i = 0; i < 8; ++i) {
      float ei = __builtin_bit_cast(float, __builtin_amdgcn_readlane(__builtin_bit_cast(int, e), i * 8));
      a += *(const f32x2*)(lds + L_OP + i * 512 + lane * 8) * ei;
    }
    stg(rws, WS_OG + hs * 1024 + lane * 16, a.x, a.y, ta, tb);
    stg(rws, lane == 0 ? WS_MG + hs * 128 : OOB, M, L, ta, tb);
  }
  MEMFENCE(); SB();
  const u32 hp = h * 8 + (lane >> 3);
  const u32 ooff = WS_OG + hp * 1024 + (w * 8 + (lane & 7)) * 16;
  const u32 poff = (lane & 4) ? WS_MG + hp * 128 : WS_PG + hp * 512 + (w * 4 + (lane & 3)) * 16;
  u32x4 og, pg;
  for (int it = 0; it < SPIN_CAP; ++it) {
    MEMFENCE();
    og = ldg(rws, ooff);
    pg = ldg(rws, poff);
    if (wave_ok(og, ta, tb) && wave_ok(pg, ta, tb)) break;
  }
  MEMFENCE();
  STAMP(6);
  {
    const float p0 = __uint_as_float(pg.x), p1 = __uint_as_float(pg.z);
    const float mo = dpp<0x141>(p0), lo_ = dpp<0x141>(p1);
    const float ms = (lane & 4) ? p0 : mo, ls = (lane & 4) ? p1 : lo_;
    const float M = smax8(ms);
    const float e = __expf(ms - M);
    const float L = ssum8(ls * e);
    const float rl = 1.0f / L;
    float o0 = ssum8(__uint_as_float(og.x) * e) * rl, o1 = ssum8(__uint_as_float(og.z) * e) * rl;
    float u0 = ssum8(p0), u1 = ssum8(p1);
    o0 = bfr(o0); o1 = bfr(o1);
    u0 = bfr(bfr(u0) + lo16(b1v)); u1 = bfr(bfr(u1) + hi16(b1v));
    const float c2 = -2.0f * 0.7978845608028654f;
    u0 = u0 / (1.0f + __expf(c2 * (u0 + 0.044715f * u0 * u0 * u0)));
    u1 = u1 / (1.0f + __expf(c2 * (u1 + 0.044715f * u1 * u1 * u1)));
    u0 = bfr(u0); u1 = bfr(u1);
    float* la = (float*)(lds + L_A) + w * 32;
    if (lane < 8) *(f32x2*)(la + 2 * lane) = f32x2{o0, o1};
    if (lane < 4) *(f32x2*)(la + 16 + 2 * lane) = mlp ? f32x2{u0, u1} : f32x2{0.f, 0.f};
    f32x4 accy = {0.f, 0.f, 0.f, 0.f};
#pragma unroll
    for (int i = 0; i < 6; ++i) accy = mma(wr[i], hilo(la[4 * i + g], low), mk4, accy);
    float* yp = (float*)(lds + L_YP) + w * 80;
#pragma unroll
    for (int j = 0; j < 4; ++j) {
      float v = accy[j] + dpp<0x128>(accy[j]);
      if (n < 8 && 4 * g + j < 10) yp[8 * (4 * g + j) + n] = v;
    }
  }
  asm volatile("s_waitcnt lgkmcnt(0)" ::: "memory");
  __builtin_amdgcn_s_barrier();
  MEMFENCE();
  STAMP(7);
  if (w == 0) {
    f32x2 a = {0.f, 0.f};
    const int t = lane < 40 ? lane : 39;
#pragma unroll
    for (int i = 0; i < 8; ++i) a += *(const f32x2*)(lds + L_YP + i * 320 + t * 8);
    stg(rws, lane < 40 ? WS_YG + (5 * s + (lane >> 3)) * 4096 + h * 128 + (lane & 7) * 16 : OOB, a.x, a.y, ta, tb);
    STAMP(8);
  } else if (w == 1 && blk < 40) {
    u32x4 y[4];
    for (int it = 0; it < SPIN_CAP; ++it) {
      MEMFENCE();
#pragma unroll
      for (int k = 0; k < 4; ++k) y[k] = ldg(rws, WS_YG + blk * 4096 + k * 1024 + lane * 16);
      if (wave_ok(y[0], ta, tb) && wave_ok(y[1], ta, tb) && wave_ok(y[2], ta, tb) && wave_ok(y[3], ta, tb)) break;
    }
    MEMFENCE();
    float y0 = (__uint_as_float(y[0].x) + __uint_as_float(y[1].x)) + (__uint_as_float(y[2].x) + __uint_as_float(y[3].x));
    float y1 = (__uint_as_float(y[0].z) + __uint_as_float(y[1].z)) + (__uint_as_float(y[2].z) + __uint_as_float(y[3].z));
    y0 = ssum8(y0) + lo16(fxv) + lo16(fbv);
    y1 = ssum8(y1) + hi16(fxv) + hi16(fbv);
    if (lane < 8) *(u32*)(out + blk * 16 + 2 * lane) = bfbits(y0) | (bfbits(y1) << 16);
    if (blk == 0 && lane == 0) __hip_atomic_store((u64*)ws, ep + 1, __ATOMIC_RELAXED, __HIP_MEMORY_SCOPE_AGENT);
    STAMP(8);
  }
}

extern "C" void kernel_launch(void* const* d_in, const int* in_sizes, int n_in, void* d_out, int out_size,
                              void* d_ws, size_t ws_size, hipStream_t stream) {
  typedef const unsigned short* P;
  (void)hipFuncSetAttribute((const void*)fused_decode_kernel, hipFuncAttributeMaxDynamicSharedMemorySize, LDS_BYTES);
  fused_decode_kernel<<<256, 512, LDS_BYTES, stream>>>((P)d_in[0], (P)d_in[1], (P)d_in[2], (P)d_in[3], (P)d_in[4],
                                                       (P)d_in[6], (P)d_in[7], (P)d_in[8], (P)d_in[9],
                                                       (unsigned short*)d_out, (char*)d_ws);
}
```

```cpp
#include <hip/hip_runtime.h>
#include <hip/hip_bf16.h>
#include <stdint.h>

#define MIMIC 0

typedef unsigned u32;
typedef unsigned long long u64;
typedef u32 u32x4 __attribute__((ext_vector_type(4)));
typedef u32 u32x2 __attribute__((ext_vector_type(2)));
typedef float f32x4 __attribute__((ext_vector_type(4)));
typedef float f32x2 __attribute__((ext_vector_type(2)));
typedef __bf16 bf16x8 __attribute__((ext_vector_type(8)));
typedef __bf16 bf16x2 __attribute__((ext_vector_type(2)));
typedef __amdgpu_buffer_rsrc_t rsrc_t;

constexpr int D = 640, NH = 32, DH = 128, SK = 8192, WIN = 2048, FF = 1728;
constexpr int AUX_NT = 2, AUX_SC1 = 16;
constexpr u32 OOB = 0x80000000u;
constexpr u32 WS_QG = 4096;
constexpr u32 WS_OG = WS_QG + 256 * 1024;
constexpr u32 WS_PG = WS_OG + 256 * 1024;
constexpr u32 WS_MG = WS_PG + 256 * 512;
constexpr u32 WS_YG = WS_MG + 256 * 128;
constexpr u32 WS_END = WS_YG + 40 * 4096;
constexpr int SPIN_CAP = 1 << 18;
constexpr int L_X = 0;
constexpr int L_QP = L_X + 8 * 1280;
constexpr int L_PP = L_QP + 4096;
constexpr int L_Q = L_PP + 4096;
constexpr int L_OP = L_Q + 256;
constexpr int L_ML = L_OP + 4096;
constexpr int L_YP = L_ML + 64;
constexpr int L_A = L_YP + 8 * 320;
constexpr int LDS_BYTES = 84 * 1024;

#define SB() __builtin_amdgcn_sched_barrier(0)
#define MEMFENCE() asm volatile("" ::: "memory")

__device__ __forceinline__ rsrc_t mk(const void* p, u32 n) {
  return __builtin_amdgcn_make_buffer_rsrc((void*)p, 0, n, 0x00020000);
}
__device__ __forceinline__ u32x4 ld16(rsrc_t r, u32 v, u32 s, const int aux_nt) {
  return aux_nt ? __builtin_amdgcn_raw_buffer_load_b128(r, v, s, AUX_NT) : __builtin_amdgcn_raw_buffer_load_b128(r, v, s, 0);
}
__device__ __forceinline__ u32x4 ldg(rsrc_t r, u32 v) { return __builtin_amdgcn_raw_buffer_load_b128(r, v, 0, AUX_SC1); }
__device__ __forceinline__ void stg(rsrc_t r, u32 v, float a, float b, u32 ta, u32 tb) {
  u32x4 g = {__float_as_uint(a), ta, __float_as_uint(b), tb};
  __builtin_amdgcn_raw_buffer_store_b128(g, r, v, 0, AUX_SC1);
}
__device__ __forceinline__ bool wave_ok(u32x4 g, u32 ta, u32 tb) {
  return __builtin_amdgcn_ballot_w64(g.y != ta || g.w != tb) == 0;
}
template <int CTRL> __device__ __forceinline__ float dpp(float x) {
  return __builtin_bit_cast(float, __builtin_amdgcn_update_dpp(0, __builtin_bit_cast(int, x), CTRL, 0xf, 0xf, true));
}
__device__ __forceinline__ float rsum16(float x) {
  x += dpp<0x128>(x); x += dpp<0x124>(x); x += dpp<0x122>(x); x += dpp<0x121>(x); return x;
}
__device__ __forceinline__ float xsum16(float x) {
  auto r = __builtin_amdgcn_permlane16_swap(__float_as_uint(x), __float_as_uint(x), false, false);
  return __uint_as_float(r[0]) + __uint_as_float(r[1]);
}
__device__ __forceinline__ float xsum32(float x) {
  auto r = __builtin_amdgcn_permlane32_swap(__float_as_uint(x), __float_as_uint(x), false, false);
  return __uint_as_float(r[0]) + __uint_as_float(r[1]);
}
__device__ __forceinline__ float xmax16(float x) {
  auto r = __builtin_amdgcn_permlane16_swap(__float_as_uint(x), __float_as_uint(x), false, false);
  return fmaxf(__uint_as_float(r[0]), __uint_as_float(r[1]));
}
__device__ __forceinline__ float xmax32(float x) {
  auto r = __builtin_amdgcn_permlane32_swap(__float_as_uint(x), __float_as_uint(x), false, false);
  return fmaxf(__uint_as_float(r[0]), __uint_as_float(r[1]));
}
__device__ __forceinline__ float ssum8(float x) { x += dpp<0x128>(x); return xsum32(xsum16(x)); }
__device__ __forceinline__ float smax8(float x) { x = fmaxf(x, dpp<0x128>(x)); return xmax32(xmax16(x)); }
__device__ __forceinline__ u32 bfbits(float x) {
  return (u32)__builtin_bit_cast(unsigned short, (__bf16)x);
}
__device__ __forceinline__ float bfr(float x) { return __uint_as_float(bfbits(x) << 16); }
__device__ __forceinline__ float lo16(u32 x) { return __uint_as_float(x << 16); }
__device__ __forceinline__ float hi16(u32 x) { return __uint_as_float(x & 0xffff0000u); }
__device__ __forceinline__ float dot2(u32 a, u32 b, float c) {
  return __builtin_amdgcn_fdot2_f32_bf16(__builtin_bit_cast(bf16x2, a), __builtin_bit_cast(bf16x2, b), c, false);
}
__device__ __forceinline__ f32x4 mma(u32x4 a, u32 bits2, const u32 (&mk4)[4], f32x4 acc) {
  u32x4 b = {bits2 & mk4[0], bits2 & mk4[1], bits2 & mk4[2], bits2 & mk4[3]};
  return __builtin_amdgcn_mfma_f32_16x16x32_bf16(__builtin_bit_cast(bf16x8, a), __builtin_bit_cast(bf16x8, b), acc, 0, 0, 0);
}
__device__ __forceinline__ u32 hilo(float v, bool low) {
  float h = bfr(v);
  u32 b = bfbits(low ? v - h : v);
  return b | (b << 16);
}

#define STAMP(i)

__global__ void __launch_bounds__(512, 2)
fused_decode_kernel(const unsigned short* __restrict__ x, const unsigned short* __restrict__ kc,
                    const unsigned short* __restrict__ vc, const unsigned short* __restrict__ wq,
                    const unsigned short* __restrict__ wo, const unsigned short* __restrict__ w1,
                    const unsigned short* __restrict__ b1, const unsigned short* __restrict__ w2,
                    const unsigned short* __restrict__ b2, unsigned short* __restrict__ out, char* ws) {
  extern __shared__ __attribute__((aligned(16))) char lds[];
  const int lane = threadIdx.x & 63;
  const int w = __builtin_amdgcn_readfirstlane(threadIdx.x >> 6);
  const int blk = blockIdx.x;
  const int h = (blk >> 6) * 8 + (blk & 7), s = (blk >> 3) & 7;
  const int n = lane & 15, g = lane >> 4;
  const bool mlp = h < FF / 64;
  STAMP(0);

  u64 ep;
  asm volatile("s_load_dwordx2 %0, %1, 0x0" : "=s"(ep) : "s"(ws) : "memory");

  const rsrc_t rx = mk(x, D * 2);
  const rsrc_t rwq = mk(wq + (size_t)(80 * s + 10 * w) * 4096 + h * 128, 10 * 8192);
  const rsrc_t rw1 = mk(w1 + (size_t)(80 * s + 10 * w) * FF + (mlp ? h : 0) * 64, mlp ? 9 * FF * 2 + 128 : 0);
  const size_t kvo = ((size_t)h * SK + (SK - WIN) + 256 * s + 4 * w) * DH;
  const rsrc_t rk = mk(kc + kvo, 65536 - 1024 * w), rv = mk(vc + kvo, 65536 - 1024 * w);
  const rsrc_t rwo = mk(wo + (size_t)(h * 128 + 16 * w) * D + 80 * s, 15 * 1280 + 160);
  const rsrc_t rw2 = mk(w2 + (size_t)((mlp ? h : 0) * 64 + 8 * w) * D + 80 * s, mlp ? 7 * 1280 + 160 : 0);
  const rsrc_t rws = mk(ws, WS_END);

  u32x4 xa = ld16(rx, lane * 16, 0, 0);
  u32x4 xb = ld16(rx, 1024 + lane * 16, 0, 0);
  SB();
  u32x4 aq[3], a1[2];
  aq[0] = ld16(rwq, g * 8192 + n * 16, 0, 1);
  aq[1] = ld16(rwq, (4 + g) * 8192 + n * 16, 0, 1);
  aq[2] = ld16(rwq, g < 2 ? (8 + g) * 8192 + n * 16 : OOB, 0, 1);
  a1[0] = ld16(rw1, (lane >> 3) * (FF * 2) + (lane & 7) * 16, 0, 1);
  a1[1] = ld16(rw1, lane < 16 ? (8 + (lane >> 3)) * (FF * 2) + (lane & 7) * 16 : OOB, 0, 1);
  SB();
  u32x4 kr[8], vr[8], wr[6];
#pragma unroll
  for (int i = 0; i < 4; ++i) kr[i] = ld16(rk, lane * 16, i * 8192, 1);
  SB();
  const u32 b1v = __builtin_amdgcn_raw_buffer_load_b32(mk(b1, mlp ? FF * 2 : 0), (h * 64 + 8 * w + 2 * (lane & 3)) * 2, 0, 0);
  const u32 fxv = __builtin_amdgcn_raw_buffer_load_b32(rx, (blk * 16 + 2 * (lane & 7)) * 2, 0, 0);
  const u32 fbv = __builtin_amdgcn_raw_buffer_load_b32(mk(b2, D * 2), (blk * 16 + 2 * (lane & 7)) * 2, 0, 0);
  SB();

  u32 mk4[4];
#pragma unroll
  for (int j = 0; j < 4; ++j) mk4[j] = (((n & 7) >> 1) == j) ? ((n & 1) ? 0xffff0000u : 0x0000ffffu) : 0u;
  const bool low = n >= 8;

  char* lx = lds + L_X + w * 1280;
  *(u32x4*)(lx + lane * 16) = xa;
  if (lane < 16) *(u32x4*)(lx + 1024 + lane * 16) = xb;
  float sm = 0.f;
#pragma unroll
  for (int j = 0; j < 4; ++j) sm += (lo16(xa[j]) + hi16(xa[j])) + (lo16(xb[j]) + hi16(xb[j]));
  const float mean = xsum32(xsum16(rsum16(sm))) * (1.0f / D);
  float sq = 0.f, sq2 = 0.f;
#pragma unroll
  for (int j = 0; j < 4; ++j) {
    float u0 = lo16(xa[j]) - mean, u1 = hi16(xa[j]) - mean, u2 = lo16(xb[j]) - mean, u3 = hi16(xb[j]) - mean;
    sq += u0 * u0 + u1 * u1; sq2 += u2 * u2 + u3 * u3;
  }
  sq += lane < 16 ? sq2 : 0.f;
  const float var = xsum32(xsum16(rsum16(sq))) * (1.0f / D);
  const float mu = mean, den = sqrtf(var + 1e-5f);
#define XN(r) ((lo16(*(const unsigned short*)(lx + 2 * (r))) - mu) / den)
  const int r0 = 80 * s + 10 * w;
  const float xq0 = XN(r0 + g), xq1 = XN(r0 + 4 + g), xq2 = g < 2 ? XN(r0 + 8 + (g & 1)) : 0.f;
  const float xp0 = XN(r0 + 2 * g + (n >> 3)), xp1 = g == 0 ? XN(r0 + 8 + (n >> 3)) : 0.f;
  f32x4 accq = {0.f, 0.f, 0.f, 0.f}, accp = {0.f, 0.f, 0.f, 0.f};
  accq = mma(aq[0], hilo(xq0, low), mk4, accq);
  accq = mma(aq[1], hilo(xq1, low), mk4, accq);
  accq = mma(aq[2], hilo(xq2, low), mk4, accq);
  accp = mma(a1[0], hilo(xp0, false), mk4, accp);
  accp = mma(a1[1], hilo(xp1, false), mk4, accp);
  accp = mma(a1[0], hilo(xp0, true), mk4, accp);
  accp = mma(a1[1], hilo(xp1, true), mk4, accp);
  STAMP(1);
  {
    float* qp = (float*)(lds + L_QP) + w * 128;
    float* pp = (float*)(lds + L_PP) + (2 * w + (g >> 1)) * 64;
#pragma unroll
    for (int j = 0; j < 4; ++j) {
      float v = accq[j] + dpp<0x128>(accq[j]);
      if (n < 8) qp[8 * (4 * g + j) + n] = v;
      if ((g >> 1) == (n >> 3)) pp[8 * (4 * (g & 1) + j) + (n & 7)] = accp[j];
    }
  }
  asm volatile("s_waitcnt lgkmcnt(0)" : "+s"(ep) :: "memory");
  u64 z = (ep + 1) * 0x9E3779B97F4A7C15ull;
  z = (z ^ (z >> 30)) * 0xBF58476D1CE4E5B9ull; z = (z ^ (z >> 27)) * 0x94D049BB133111EBull; z ^= z >> 31;
  const u32 ta = (u32)z, tb = (u32)(z >> 32);
  __builtin_amdgcn_s_barrier();
  MEMFENCE();
  const u32 hs = h * 8 + s;
  if (w == 0) {
    f32x2 a = {0.f, 0.f};
#pragma unroll
    for (int i = 0; i < 8; ++i) a += *(const f32x2*)(lds + L_QP + i * 512 + lane * 8);
    stg(rws, WS_QG + hs * 1024 + lane * 16, a.x, a.y, ta, tb);
  } else if (w == 1) {
    f32x2 a = {0.f, 0.f};
#pragma unroll
    for (int i = 0; i < 8; ++i) a += *(const f32x2*)(lds + L_PP + ((lane >> 5) * 8 + i) * 256 + (lane & 31) * 8);
    a.x = xsum32(a.x); a.y = xsum32(a.y);
    stg(rws, lane < 32 ? WS_PG + hs * 512 + lane * 16 : OOB, a.x, a.y, ta, tb);
  }
  MEMFENCE(); SB();
  STAMP(2);
#pragma unroll
  for (int i = 4; i < 8; ++i) kr[i] = ld16(rk, lane * 16, i * 8192, 1);
  SB();
  const u32 qoff = WS_QG + (h * 8 + (lane >> 3)) * 1024 + (w * 8 + (lane & 7)) * 16;
  u32x4 qg = ldg(rws, qoff);
  SB();
#pragma unroll
  for (int i = 0; i < 4; ++i) vr[i] = ld16(rv, lane * 16, i * 8192, 1);
  SB();
  if (!wave_ok(qg, ta, tb)) {
    for (int it = 0; it < SPIN_CAP; ++it) {
      MEMFENCE();
      qg = ldg(rws, qoff);
      if (wave_ok(qg, ta, tb)) break;
    }
  }
  MEMFENCE();
  STAMP(3);
  {
    float q0 = ssum8(__uint_as_float(qg.x)), q1 = ssum8(__uint_as_float(qg.z));
    if (lane < 8) *(u32*)(lds + L_Q + (w * 8 + lane) * 4) = bfbits(q0) | (bfbits(q1) << 16);
  }
  asm volatile("s_waitcnt lgkmcnt(0)" ::: "memory");
  __builtin_amdgcn_s_barrier();
  MEMFENCE();
  const u32x4 q = *(const u32x4*)(lds + L_Q + n * 16);
  float sc[8];
#pragma unroll
  for (int i = 0; i < 8; ++i) {
    float d = dot2(kr[i].x, q.x, 0.f);
    d = dot2(kr[i].y, q.y, d); d = dot2(kr[i].z, q.z, d); d = dot2(kr[i].w, q.w, d);
    d = rsum16(d);
    sc[i] = d * 0.08838834764831845f;
    if (i & 1) { SB(); vr[4 + (i >> 1)] = ld16(rv, lane * 16, (4 + (i >> 1)) * 8192, 1); SB(); }
  }
  float m = fmaxf(fmaxf(fmaxf(sc[0], sc[1]), fmaxf(sc[2], sc[3])), fmaxf(fmaxf(sc[4], sc[5]), fmaxf(sc[6], sc[7])));
  m = xmax32(xmax16(m));
  float l = 0.f;
  u32 pb[8];
#pragma unroll
  for (int i = 0; i < 8; ++i) {
    float p = __expf(sc[i] - m);
    l += p;
    pb[i] = hilo(p, low);
  }
  l = xsum32(xsum16(l));
  STAMP(4);
  const u32 woff = n < 10 ? g * 1280 + n * 16 : OOB;
  f32x4 acco = {0.f, 0.f, 0.f, 0.f};
#pragma unroll
  for (int i = 0; i < 8; ++i) {
    acco = mma(vr[i], pb[i], mk4, acco);
    SB();
    if (i < 4) wr[i] = ld16(rwo, woff + i * 5120, 0, 1);
    else if (i < 6) wr[i] = ld16(rw2, woff + (i - 4) * 5120, 0, 1);
    SB();
  }
  {
    float* op = (float*)(lds + L_OP) + w * 128;
#pragma unroll
    for (int j = 0; j < 4; ++j) {
      float v = acco[j] + dpp<0x128>(acco[j]);
      if (n < 8) op[8 * (4 * g + j) + n] = v;
    }
    if (lane == 0) *(f32x2*)(lds + L_ML + w * 8) = f32x2{m, l};
  }
  asm volatile("s_waitcnt lgkmcnt(0)" ::: "memory");
  STAMP(5);
  __builtin_amdgcn_s_barrier();
  MEMFENCE();
  if (w == 0) {
    f32x2 ml = *(const f32x2*)(lds + L_ML + (lane >> 3) * 8);
    const float M = smax8(ml.x);
    const float e = __expf(ml.x - M);
    const float L = ssum8(ml.y * e);
    f32x2 a = {0.f, 0.f};
#pragma unroll
    for (int i = 0; i < 8; ++i) {
      float ei = __builtin_bit_cast(float, __builtin_amdgcn_readlane(__builtin_bit_cast(int, e), i * 8));
      a += *(const f32x2*)(lds + L_OP + i * 512 + lane * 8) * ei;
    }
    stg(rws, WS_OG + hs * 1024 + lane * 16, a.x, a.y, ta, tb);
    stg(rws, lane == 0 ? WS_MG + hs * 128 : OOB, M, L, ta, tb);
  }
  MEMFENCE(); SB();
  const u32 hp = h * 8 + (lane >> 3);
  const u32 ooff = WS_OG + hp * 1024 + (w * 8 + (lane & 7)) * 16;
  const u32 poff = (lane & 4) ? WS_MG + hp * 128 : WS_PG + hp * 512 + (w * 4 + (lane & 3)) * 16;
  u32x4 og, pg;
  for (int it = 0; it < SPIN_CAP; ++it) {
    MEMFENCE();
    og = ldg(rws, ooff);
    pg = ldg(rws, poff);
    if (wave_ok(og, ta, tb) && wave_ok(pg, ta, tb)) break;
  }
  MEMFENCE();
  STAMP(6);
  {
    const float p0 = __uint_as_float(pg.x), p1 = __uint_as_float(pg.z);
    const float mo = dpp<0x141>(p0), lo_ = dpp<0x141>(p1);
    const float ms = (lane & 4) ? p0 : mo, ls = (lane & 4) ? p1 : lo_;
    const float M = smax8(ms);
    const float e = __expf(ms - M);
    const float L = ssum8(ls * e);
    const float rl = 1.0f / L;
    float o0 = ssum8(__uint_as_float(og.x) * e) * rl, o1 = ssum8(__uint_as_float(og.z) * e) * rl;
    float u0 = ssum8(p0), u1 = ssum8(p1);
    u0 += lo16(b1v); u1 += hi16(b1v);
    const float c2 = -2.0f * 0.7978845608028654f;
    u0 = u0 / (1.0f + __expf(c2 * (u0 + 0.044715f * u0 * u0 * u0)));
    u1 = u1 / (1.0f + __expf(c2 * (u1 + 0.044715f * u1 * u1 * u1)));
    float* la = (float*)(lds + L_A) + w * 32;
    if (lane < 8) *(f32x2*)(la + 2 * lane) = f32x2{o0, o1};
    if (lane < 4) *(f32x2*)(la + 16 + 2 * lane) = mlp ? f32x2{u0, u1} : f32x2{0.f, 0.f};
    f32x4 accy = {0.f, 0.f, 0.f, 0.f};
#pragma unroll
    for (int i = 0; i < 6; ++i) accy = mma(wr[i], hilo(la[4 * i + g], low), mk4, accy);
    float* yp = (float*)(lds + L_YP) + w * 80;
#pragma unroll
    for (int j = 0; j < 4; ++j) {
      float v = accy[j] + dpp<0x128>(accy[j]);
      if (n < 8 && 4 * g + j < 10) yp[8 * (4 * g + j) + n] = v;
    }
  }
  asm volatile("s_waitcnt lgkmcnt(0)" ::: "memory");
  __builtin_amdgcn_s_barrier();
  MEMFENCE();
  STAMP(7);
  if (w == 0) {
    f32x2 a = {0.f, 0.f};
    const int t = lane < 40 ? lane : 39;
#pragma unroll
    for (int i = 0; i < 8; ++i) a += *(const f32x2*)(lds + L_YP + i * 320 + t * 8);
    stg(rws, lane < 40 ? WS_YG + (5 * s + (lane >> 3)) * 4096 + h * 128 + (lane & 7) * 16 : OOB, a.x, a.y, ta, tb);
    STAMP(8);
  } else if (w == 1 && blk < 40) {
    u32x4 y[4];
    for (int it = 0; it < SPIN_CAP; ++it) {
      MEMFENCE();
#pragma unroll
      for (int k = 0; k < 4; ++k) y[k] = ldg(rws, WS_YG + blk * 4096 + k * 1024 + lane * 16);
      if (wave_ok(y[0], ta, tb) && wave_ok(y[1], ta, tb) && wave_ok(y[2], ta, tb) && wave_ok(y[3], ta, tb)) break;
    }
    MEMFENCE();
    float y0 = (__uint_as_float(y[0].x) + __uint_as_float(y[1].x)) + (__uint_as_float(y[2].x) + __uint_as_float(y[3].x));
    float y1 = (__uint_as_float(y[0].z) + __uint_as_float(y[1].z)) + (__uint_as_float(y[2].z) + __uint_as_float(y[3].z));
    y0 = ssum8(y0) + lo16(fxv) + lo16(fbv);
    y1 = ssum8(y1) + hi16(fxv) + hi16(fbv);
    if (lane < 8) *(u32*)(out + blk * 16 + 2 * lane) = bfbits(y0) | (bfbits(y1) << 16);
    if (blk == 0 && lane == 0) __hip_atomic_store((u64*)ws, ep + 1, __ATOMIC_RELAXED, __HIP_MEMORY_SCOPE_AGENT);
    STAMP(8);
  }
}

extern "C" void kernel_launch(void* const* d_in, const int* in_sizes, int n_in, void* d_out, int out_size,
                              void* d_ws, size_t ws_size, hipStream_t stream) {
  typedef const unsigned short* P;
  (void)hipFuncSetAttribute((const void*)fused_decode_kernel, hipFuncAttributeMaxDynamicSharedMemorySize, LDS_BYTES);
  fused_decode_kernel<<<256, 512, LDS_BYTES, stream>>>((P)d_in[0], (P)d_in[1], (P)d_in[2], (P)d_in[3], (P)d_in[4],
                                                       (P)d_in[6], (P)d_in[7], (P)d_in[8], (P)d_in[9],
                                                       (unsigned short*)d_out, (char*)d_ws);
}
```

```cpp
#include <hip/hip_runtime.h>
#include <hip/hip_bf16.h>
#include <stdint.h>

#define MIMIC 1

typedef unsigned u32;
typedef unsigned long long u64;
typedef u32 u32x4 __attribute__((ext_vector_type(4)));
typedef u32 u32x2 __attribute__((ext_vector_type(2)));
typedef float f32x4 __attribute__((ext_vector_type(4)));
typedef float f32x2 __attribute__((ext_vector_type(2)));
typedef __bf16 bf16x8 __attribute__((ext_vector_type(8)));
typedef __bf16 bf16x2 __attribute__((ext_vector_type(2)));
typedef __amdgpu_buffer_rsrc_t rsrc_t;

constexpr int D = 640, NH = 32, DH = 128, SK = 8192, WIN = 2048, FF = 1728;
constexpr int AUX_NT = 2, AUX_SC1 = 16;
constexpr u32 OOB = 0x80000000u;
constexpr u32 WS_QG = 4096;
constexpr u32 WS_OG = WS_QG + 256 * 1024;
constexpr u32 WS_PG = WS_OG + 256 * 1024;
constexpr u32 WS_MG = WS_PG + 256 * 512;
constexpr u32 WS_YG = WS_MG + 256 * 128;
constexpr u32 WS_END = WS_YG + 40 * 4096;
constexpr int SPIN_CAP = 1 << 18;
constexpr int L_X = 0;
constexpr int L_QP = L_X + 8 * 1280;
constexpr int L_PP = L_QP + 4096;
constexpr int L_Q = L_PP + 4096;
constexpr int L_OP = L_Q + 256;
constexpr int L_ML = L_OP + 4096;
constexpr int L_YP = L_ML + 64;
constexpr int L_A = L_YP + 8 * 320;
constexpr int LDS_BYTES = 84 * 1024;

#define SB() __builtin_amdgcn_sched_barrier(0)
#define MEMFENCE() asm volatile("" ::: "memory")

__device__ __forceinline__ rsrc_t mk(const void* p, u32 n) {
  return __builtin_amdgcn_make_buffer_rsrc((void*)p, 0, n, 0x00020000);
}
__device__ __forceinline__ u32x4 ld16(rsrc_t r, u32 v, u32 s, const int aux_nt) {
  return aux_nt ? __builtin_amdgcn_raw_buffer_load_b128(r, v, s, AUX_NT) : __builtin_amdgcn_raw_buffer_load_b128(r, v, s, 0);
}
__device__ __forceinline__ u32x4 ldg(rsrc_t r, u32 v) { return __builtin_amdgcn_raw_buffer_load_b128(r, v, 0, AUX_SC1); }
__device__ __forceinline__ void stg(rsrc_t r, u32 v, float a, float b, u32 ta, u32 tb) {
  u32x4 g = {__float_as_uint(a), ta, __float_as_uint(b), tb};
  __builtin_amdgcn_raw_buffer_store_b128(g, r, v, 0, AUX_SC1);
}
__device__ __forceinline__ bool wave_ok(u32x4 g, u32 ta, u32 tb) {
  return __builtin_amdgcn_ballot_w64(g.y != ta || g.w != tb) == 0;
}
template <int CTRL> __device__ __forceinline__ float dpp(float x) {
  return __builtin_bit_cast(float, __builtin_amdgcn_update_dpp(0, __builtin_bit_cast(int, x), CTRL, 0xf, 0xf, true));
}
__device__ __forceinline__ float rsum16(float x) {
  x += dpp<0x128>(x); x += dpp<0x124>(x); x += dpp<0x122>(x); x += dpp<0x121>(x); return x;
}
__device__ __forceinline__ float xsum16(float x) {
  auto r = __builtin_amdgcn_permlane16_swap(__float_as_uint(x), __float_as_uint(x), false, false);
  return __uint_as_float(r[0]) + __uint_as_float(r[1]);
}
__device__ __forceinline__ float xsum32(float x) {
  auto r = __builtin_amdgcn_permlane32_swap(__float_as_uint(x), __float_as_uint(x), false, false);
  return __uint_as_float(r[0]) + __uint_as_float(r[1]);
}
__device__ __forceinline__ float xmax16(float x) {
  auto r = __builtin_amdgcn_permlane16_swap(__float_as_uint(x), __float_as_uint(x), false, false);
  return fmaxf(__uint_as_float(r[0]), __uint_as_float(r[1]));
}
__device__ __forceinline__ float xmax32(float x) {
  auto r = __builtin_amdgcn_permlane32_swap(__float_as_uint(x), __float_as_uint(x), false, false);
  return fmaxf(__uint_as_float(r[0]), __uint_as_float(r[1]));
}
__device__ __forceinline__ float ssum8(float x) { x += dpp<0x128>(x); return xsum32(xsum16(x)); }
__device__ __forceinline__ float smax8(float x) { x = fmaxf(x, dpp<0x128>(x)); return xmax32(xmax16(x)); }
__device__ __forceinline__ u32 bfbits(float x) {
  return (u32)__builtin_bit_cast(unsigned short, (__bf16)x);
}
__device__ __forceinline__ float bfr(float x) { return __uint_as_float(bfbits(x) << 16); }
__device__ __forceinline__ float lo16(u32 x) { return __uint_as_float(x << 16); }
__device__ __forceinline__ float hi16(u32 x) { return __uint_as_float(x & 0xffff0000u); }
__device__ __forceinline__ float dot2(u32 a, u32 b, float c) {
  return __builtin_amdgcn_fdot2_f32_bf16(__builtin_bit_cast(bf16x2, a), __builtin_bit_cast(bf16x2, b), c, false);
}
__device__ __forceinline__ f32x4 mma(u32x4 a, u32 bits2, const u32 (&mk4)[4], f32x4 acc) {
  u32x4 b = {bits2 & mk4[0], bits2 & mk4[1], bits2 & mk4[2], bits2 & mk4[3]};
  return __builtin_amdgcn_mfma_f32_16x16x32_bf16(__builtin_bit_cast(bf16x8, a), __builtin_bit_cast(bf16x8, b), acc, 0, 0, 0);
}
__device__ __forceinline__ u32 hilo(float v, bool low) {
  float h = bfr(v);
  u32 b = bfbits(low ? v - h : v);
  return b | (b << 16);
}

#define STAMP(i)

__global__ void __launch_bounds__(512, 2)
fused_decode_kernel(const unsigned short* __restrict__ x, const unsigned short* __restrict__ kc,
                    const unsigned short* __restrict__ vc, const unsigned short* __restrict__ wq,
                    const unsigned short* __restrict__ wo, const unsigned short* __restrict__ w1,
                    const unsigned short* __restrict__ b1, const unsigned short* __restrict__ w2,
                    const unsigned short* __restrict__ b2, unsigned short* __restrict__ out, char* ws) {
  extern __shared__ __attribute__((aligned(16))) char lds[];
  const int lane = threadIdx.x & 63;
  const int w = __builtin_amdgcn_readfirstlane(threadIdx.x >> 6);
  const int blk = blockIdx.x;
  const int h = (blk >> 6) * 8 + (blk & 7), s = (blk >> 3) & 7;
  const int n = lane & 15, g = lane >> 4;
  const bool mlp = h < FF / 64;
  const u64 tstart=__builtin_amdgcn_s_memrealtime();

  u64 ep;
  asm volatile("s_load_dwordx2 %0, %1, 0x0" : "=s"(ep) : "s"(ws) : "memory");

  const rsrc_t rx = mk(x, D * 2);
  const rsrc_t rwq = mk(wq + (size_t)(80 * s + 10 * w) * 4096 + h * 128, 10 * 8192);
  const rsrc_t rw1 = mk(w1 + (size_t)(80 * s + 10 * w) * FF + (mlp ? h : 0) * 64, mlp ? 9 * FF * 2 + 128 : 0);
  const size_t kvo = ((size_t)h * SK + (SK - WIN) + 256 * s + 4 * w) * DH;
  const rsrc_t rk = mk(kc + kvo, 65536 - 1024 * w), rv = mk(vc + kvo, 65536 - 1024 * w);
  const rsrc_t rwo = mk(wo + (size_t)(h * 128 + 16 * w) * D + 80 * s, 15 * 1280 + 160);
  const rsrc_t rw2 = mk(w2 + (size_t)((mlp ? h : 0) * 64 + 8 * w) * D + 80 * s, mlp ? 7 * 1280 + 160 : 0);
  const rsrc_t rws = mk(ws, WS_END);

  u32x4 xa = ld16(rx, lane * 16, 0, 0);
  u32x4 xb = ld16(rx, 1024 + lane * 16, 0, 0);
  SB();
  u32x4 aq[3], a1[2];
  aq[0] = ld16(rwq, g * 8192 + n * 16, 0, 1);
  aq[1] = ld16(rwq, (4 + g) * 8192 + n * 16, 0, 1);
  aq[2] = ld16(rwq, g < 2 ? (8 + g) * 8192 + n * 16 : OOB, 0, 1);
  a1[0] = ld16(rw1, (lane >> 3) * (FF * 2) + (lane & 7) * 16, 0, 1);
  a1[1] = ld16(rw1, lane < 16 ? (8 + (lane >> 3)) * (FF * 2) + (lane & 7) * 16 : OOB, 0, 1);
  SB();
  u32x4 kr[8], vr[8], wr[6];
#pragma unroll
  for (int i = 0; i < 4; ++i) kr[i] = ld16(rk, lane * 16, i * 8192, 1);
  SB();
  const u32 b1v = __builtin_amdgcn_raw_buffer_load_b32(mk(b1, mlp ? FF * 2 : 0), (h * 64 + 8 * w + 2 * (lane & 3)) * 2, 0, 0);
  const u32 fxv = __builtin_amdgcn_raw_buffer_load_b32(rx, (blk * 16 + 2 * (lane & 7)) * 2, 0, 0);
  const u32 fbv = __builtin_amdgcn_raw_buffer_load_b32(mk(b2, D * 2), (blk * 16 + 2 * (lane & 7)) * 2, 0, 0);
  SB();

  u32 mk4[4];
#pragma unroll
  for (int j = 0; j < 4; ++j) mk4[j] = (((n & 7) >> 1) == j) ? ((n & 1) ? 0xffff0000u : 0x0000ffffu) : 0u;
  const bool low = n >= 8;

  char* lx = lds + L_X + w * 1280;
  *(u32x4*)(lx + lane * 16) = xa;
  if (lane < 16) *(u32x4*)(lx + 1024 + lane * 16) = xb;
  float sm = 0.f;
#pragma unroll
  for (int j = 0; j < 4; ++j) sm += (lo16(xa[j]) + hi16(xa[j])) + (lo16(xb[j]) + hi16(xb[j]));
  const float mean = xsum32(xsum16(rsum16(sm))) * (1.0f / D);
  float sq = 0.f, sq2 = 0.f;
#pragma unroll
  for (int j = 0; j < 4; ++j) {
    float u0 = lo16(xa[j]) - mean, u1 = hi16(xa[j]) - mean, u2 = lo16(xb[j]) - mean, u3 = hi16(xb[j]) - mean;
    sq += u0 * u0 + u1 * u1; sq2 += u2 * u2 + u3 * u3;
  }
  sq += lane < 16 ? sq2 : 0.f;
  const float var = xsum32(xsum16(rsum16(sq))) * (1.0f / D);
  const float mu = bfr(mean), den = bfr(sqrtf(bfr(bfr(var) + 1e-5f)));
#define XN(r) bfr(bfr(lo16(*(const unsigned short*)(lx + 2 * (r))) - mu) / den)
  const int r0 = 80 * s + 10 * w;
  const float xq0 = XN(r0 + g), xq1 = XN(r0 + 4 + g), xq2 = g < 2 ? XN(r0 + 8 + (g & 1)) : 0.f;
  const float xp0 = XN(r0 + 2 * g + (n >> 3)), xp1 = g == 0 ? XN(r0 + 8 + (n >> 3)) : 0.f;
  f32x4 accq = {0.f, 0.f, 0.f, 0.f}, accp = {0.f, 0.f, 0.f, 0.f};
  accq = mma(aq[0], hilo(xq0, low), mk4, accq);
  accq = mma(aq[1], hilo(xq1, low), mk4, accq);
  accq = mma(aq[2], hilo(xq2, low), mk4, accq);
  accp = mma(a1[0], hilo(xp0, false), mk4, accp);
  accp = mma(a1[1], hilo(xp1, false), mk4, accp);
  STAMP(1);
  {
    float* qp = (float*)(lds + L_QP) + w * 128;
    float* pp = (float*)(lds + L_PP) + (2 * w + (g >> 1)) * 64;
#pragma unroll
    for (int j = 0; j < 4; ++j) {
      float v = accq[j] + dpp<0x128>(accq[j]);
      if (n < 8) qp[8 * (4 * g + j) + n] = v;
      if ((g >> 1) == (n >> 3)) pp[8 * (4 * (g & 1) + j) + (n & 7)] = accp[j];
    }
  }
  asm volatile("s_waitcnt lgkmcnt(0)" : "+s"(ep) :: "memory");
  u64 z = (ep + 1) * 0x9E3779B97F4A7C15ull;
  z = (z ^ (z >> 30)) * 0xBF58476D1CE4E5B9ull; z = (z ^ (z >> 27)) * 0x94D049BB133111EBull; z ^= z >> 31;
  const u32 ta = (u32)z, tb = (u32)(z >> 32);
  __builtin_amdgcn_s_barrier();
  MEMFENCE();
  const u32 hs = h * 8 + s;
  if (w == 0) {
    f32x2 a = {0.f, 0.f};
#pragma unroll
    for (int i = 0; i < 8; ++i) a += *(const f32x2*)(lds + L_QP + i * 512 + lane * 8);
    stg(rws, WS_QG + hs * 1024 + lane * 16, a.x, a.y, ta, tb);
  } else if (w == 1) {
    f32x2 a = {0.f, 0.f};
#pragma unroll
    for (int i = 0; i < 8; ++i) a += *(const f32x2*)(lds + L_PP + ((lane >> 5) * 8 + i) * 256 + (lane & 31) * 8);
    a.x = xsum32(a.x); a.y = xsum32(a.y);
    stg(rws, lane < 32 ? WS_PG + hs * 512 + lane * 16 : OOB, a.x, a.y, ta, tb);
  }
  MEMFENCE(); SB();
  STAMP(2);
#pragma unroll
  for (int i = 4; i < 8; ++i) kr[i] = ld16(rk, lane * 16, i * 8192, 1);
  SB();
  const u32 qoff = WS_QG + (h * 8 + (lane >> 3)) * 1024 + (w * 8 + (lane & 7)) * 16;
  u32x4 qg = ldg(rws, qoff);
  SB();
#pragma unroll
  for (int i = 0; i < 4; ++i) vr[i] = ld16(rv, lane * 16, i * 8192, 1);
  SB();
  if (!wave_ok(qg, ta, tb)) {
    for (int it = 0; it < SPIN_CAP; ++it) {
      MEMFENCE();
      qg = ldg(rws, qoff);
      if (wave_ok(qg, ta, tb)) break;
    }
  }
  MEMFENCE();
  STAMP(3);
  {
    float q0 = ssum8(__uint_as_float(qg.x)), q1 = ssum8(__uint_as_float(qg.z));
    if (lane < 8) *(u32*)(lds + L_Q + (w * 8 + lane) * 4) = bfbits(q0) | (bfbits(q1) << 16);
  }
  asm volatile("s_waitcnt lgkmcnt(0)" ::: "memory");
  __builtin_amdgcn_s_barrier();
  MEMFENCE();
  const u32x4 q = *(const u32x4*)(lds + L_Q + n * 16);
  float sc[8];
#pragma unroll
  for (int i = 0; i < 8; ++i) {
    float d = dot2(kr[i].x, q.x, 0.f);
    d = dot2(kr[i].y, q.y, d); d = dot2(kr[i].z, q.z, d); d = dot2(kr[i].w, q.w, d);
    d = rsum16(d);
    sc[i] = bfr(bfr(d) * 0.08838834764831845f);
    if (i & 1) { SB(); vr[4 + (i >> 1)] = ld16(rv, lane * 16, (4 + (i >> 1)) * 8192, 1); SB(); }
  }
  float m = fmaxf(fmaxf(fmaxf(sc[0], sc[1]), fmaxf(sc[2], sc[3])), fmaxf(fmaxf(sc[4], sc[5]), fmaxf(sc[6], sc[7])));
  m = xmax32(xmax16(m));
  float l = 0.f;
  u32 pb[8];
#pragma unroll
  for (int i = 0; i < 8; ++i) {
    float p = __expf(sc[i] - m);
    l += p;
    pb[i] = hilo(p, low);
  }
  l = xsum32(xsum16(l));
  STAMP(4);
  const u32 woff = n < 10 ? g * 1280 + n * 16 : OOB;
  f32x4 acco = {0.f, 0.f, 0.f, 0.f};
#pragma unroll
  for (int i = 0; i < 8; ++i) {
    acco = mma(vr[i], pb[i], mk4, acco);
    SB();
    if (i < 4) wr[i] = ld16(rwo, woff + i * 5120, 0, 1);
    else if (i < 6) wr[i] = ld16(rw2, woff + (i - 4) * 5120, 0, 1);
    SB();
  }
  {
    float* op = (float*)(lds + L_OP) + w * 128;
#pragma unroll
    for (int j = 0; j < 4; ++j) {
      float v = acco[j] + dpp<0x128>(acco[j]);
      if (n < 8) op[8 * (4 * g + j) + n] = v;
    }
    if (lane == 0) *(f32x2*)(lds + L_ML + w * 8) = f32x2{m, l};
  }
  asm volatile("s_waitcnt lgkmcnt(0)" ::: "memory");
  STAMP(5);
  __builtin_amdgcn_s_barrier();
  MEMFENCE();
  if (w == 0) {
    f32x2 ml = *(const f32x2*)(lds + L_ML + (lane >> 3) * 8);
    const float M = smax8(ml.x);
    const float e = __expf(ml.x - M);
    const float L = ssum8(ml.y * e);
    f32x2 a = {0.f, 0.f};
#pragma unroll
    for (int i = 0; i < 8; ++i) {
      float ei = __builtin_bit_cast(float, __builtin_amdgcn_readlane(__builtin_bit_cast(int, e), i * 8));
      a += *(const f32x2*)(lds + L_OP + i * 512 + lane * 8) * ei;
    }
    stg(rws, WS_OG + hs * 1024 + lane * 16, a.x, a.y, ta, tb);
    stg(rws, lane == 0 ? WS_MG + hs * 128 : OOB, M, L, ta, tb);
  }
  MEMFENCE(); SB();
  const u32 hp = h * 8 + (lane >> 3);
  const u32 ooff = WS_OG + hp * 1024 + (w * 8 + (lane & 7)) * 16;
  const u32 poff = (lane & 4) ? WS_MG + hp * 128 : WS_PG + hp * 512 + (w * 4 + (lane & 3)) * 16;
  u32x4 og, pg;
  for (int it = 0; it < SPIN_CAP; ++it) {
    MEMFENCE();
    og = ldg(rws, ooff);
    pg = ldg(rws, poff);
    if (wave_ok(og, ta, tb) && wave_ok(pg, ta, tb)) break;
  }
  MEMFENCE();
  STAMP(6);
  {
    const float p0 = __uint_as_float(pg.x), p1 = __uint_as_float(pg.z);
    const float mo = dpp<0x141>(p0), lo_ = dpp<0x141>(p1);
    const float ms = (lane & 4) ? p0 : mo, ls = (lane & 4) ? p1 : lo_;
    const float M = smax8(ms);
    const float e = __expf(ms - M);
    const float L = ssum8(ls * e);
    const float rl = 1.0f / L;
    float o0 = ssum8(__uint_as_float(og.x) * e) * rl, o1 = ssum8(__uint_as_float(og.z) * e) * rl;
    float u0 = ssum8(p0), u1 = ssum8(p1);
    o0 = bfr(o0); o1 = bfr(o1);
    u0 = bfr(bfr(u0) + lo16(b1v)); u1 = bfr(bfr(u1) + hi16(b1v));
    const float c2 = -2.0f * 0.7978845608028654f;
    u0 = u0 / (1.0f + __expf(c2 * (u0 + 0.044715f * u0 * u0 * u0)));
    u1 = u1 / (1.0f + __expf(c2 * (u1 + 0.044715f * u1 * u1 * u1)));
    u0 = bfr(u0); u1 = bfr(u1);
    float* la = (float*)(lds + L_A) + w * 32;
    if (lane < 8) *(f32x2*)(la + 2 * lane) = f32x2{o0, o1};
    if (lane < 4) *(f32x2*)(la + 16 + 2 * lane) = mlp ? f32x2{u0, u1} : f32x2{0.f, 0.f};
    f32x4 accy = {0.f, 0.f, 0.f, 0.f};
#pragma unroll
    for (int i = 0; i < 6; ++i) accy = mma(wr[i], hilo(la[4 * i + g], low), mk4, accy);
    float* yp = (float*)(lds + L_YP) + w * 80;
#pragma unroll
    for (int j = 0; j < 4; ++j) {
      float v = accy[j] + dpp<0x128>(accy[j]);
      if (n < 8 && 4 * g + j < 10) yp[8 * (4 * g + j) + n] = v;
    }
  }
  asm volatile("s_waitcnt lgkmcnt(0)" ::: "memory");
  __builtin_amdgcn_s_barrier();
  MEMFENCE();
  STAMP(7);
  if (w == 0) {
    f32x2 a = {0.f, 0.f};
    const int t = lane < 40 ? lane : 39;
#pragma unroll
    for (int i = 0; i < 8; ++i) a += *(const f32x2*)(lds + L_YP + i * 320 + t * 8);
    stg(rws, lane < 40 ? WS_YG + (5 * s + (lane >> 3)) * 4096 + h * 128 + (lane & 7) * 16 : OOB, a.x, a.y, ta, tb);
    STAMP(8);
  } else if (w == 1 && blk < 40) {
    u32x4 y[4];
    for (int it = 0; it < SPIN_CAP; ++it) {
      MEMFENCE();
#pragma unroll
      for (int k = 0; k < 4; ++k) y[k] = ldg(rws, WS_YG + blk * 4096 + k * 1024 + lane * 16);
      if (wave_ok(y[0], ta, tb) && wave_ok(y[1], ta, tb) && wave_ok(y[2], ta, tb) && wave_ok(y[3], ta, tb)) break;
    }
    MEMFENCE();
    float y0 = (__uint_as_float(y[0].x) + __uint_as_float(y[1].x)) + (__uint_as_float(y[2].x) + __uint_as_float(y[3].x));
    float y1 = (__uint_as_float(y[0].z) + __uint_as_float(y[1].z)) + (__uint_as_float(y[2].z) + __uint_as_float(y[3].z));
    y0 = ssum8(y0) + lo16(fxv) + lo16(fbv);
    y1 = ssum8(y1) + hi16(fxv) + hi16(fbv);
    while(__builtin_amdgcn_s_memrealtime()-tstart < 2500) {}
    if (lane < 8) *(u32*)(out + blk * 16 + 2 * lane) = bfbits(y0) | (bfbits(y1) << 16);
    if (blk == 0 && lane == 0) __hip_atomic_store((u64*)ws, ep + 1, __ATOMIC_RELAXED, __HIP_MEMORY_SCOPE_AGENT);
    STAMP(8);
  }
}

extern "C" void kernel_launch(void* const* d_in, const int* in_sizes, int n_in, void* d_out, int out_size,
                              void* d_ws, size_t ws_size, hipStream_t stream) {
  typedef const unsigned short* P;
  (void)hipFuncSetAttribute((const void*)fused_decode_kernel, hipFuncAttributeMaxDynamicSharedMemorySize, LDS_BYTES);
  fused_decode_kernel<<<256, 512, LDS_BYTES, stream>>>((P)d_in[0], (P)d_in[1], (P)d_in[2], (P)d_in[3], (P)d_in[4],
                                                       (P)d_in[6], (P)d_in[7], (P)d_in[8], (P)d_in[9],
                                                       (unsigned short*)d_out, (char*)d_ws);
}
```

```cpp
#include <hip/hip_runtime.h>
#include <hip/hip_bf16.h>
#include <stdint.h>

#define WN 4
#define WA 2
#define PP 6
#define DLY 22
#define PIPE 5
#define PIPE2 1
typedef unsigned u32;
typedef unsigned long long u64;
typedef u32 u32x4 __attribute__((ext_vector_type(4)));
typedef u32 u32x2 __attribute__((ext_vector_type(2)));
typedef float f32x4 __attribute__((ext_vector_type(4)));
typedef float f32x2 __attribute__((ext_vector_type(2)));
typedef __bf16 bf16x8 __attribute__((ext_vector_type(8)));
typedef __bf16 bf16x2 __attribute__((ext_vector_type(2)));
typedef __amdgpu_buffer_rsrc_t rsrc_t;

constexpr int D = 640, NH = 32, DH = 128, SK = 8192, WIN = 2048, FF = 1728;
constexpr int AUX_NT = 2, AUX_SC1 = 16;
constexpr u32 OOB = 0x80000000u;
constexpr u32 WS_QG = 4096;
constexpr u32 WS_OG = WS_QG + 256 * 1024;
constexpr u32 WS_PG = WS_OG + 256 * 1024;
constexpr u32 WS_YG = WS_PG + 256 * 512;
constexpr u32 WS_END = WS_YG + 40 * 4096;
constexpr int SPIN_CAP = 1 << 18;
constexpr int L_X = 0;
constexpr int L_QP = L_X + 8 * 2048;
constexpr int L_PP = L_QP + 4096;
constexpr int L_Q = L_PP + 4096;
constexpr int L_OP = L_Q + 256;
constexpr int L_M = L_OP + 4096;
constexpr int L_L = L_M + 32;
constexpr int L_YP = L_L + 32;
constexpr int L_DUMMY = L_YP + 4096;
constexpr int L_K = 36 * 1024;
constexpr int LDS_BYTES = L_K + 65536;

#define SB() __builtin_amdgcn_sched_barrier(0)
#define MEMFENCE() asm volatile("" ::: "memory")

__device__ __forceinline__ rsrc_t mk(const void* p, u32 n) {
  return __builtin_amdgcn_make_buffer_rsrc((void*)p, 0, n, 0x00020000);
}
__device__ __forceinline__ u32x4 ld16(rsrc_t r, u32 v, u32 s, const int aux_nt) {
  return aux_nt ? __builtin_amdgcn_raw_buffer_load_b128(r, v, s, AUX_NT) : __builtin_amdgcn_raw_buffer_load_b128(r, v, s, 0);
}
__device__ __forceinline__ u32x4 ldg(rsrc_t r, u32 v) { return __builtin_amdgcn_raw_buffer_load_b128(r, v, 0, AUX_SC1); }
__device__ __forceinline__ void stg(rsrc_t r, u32 v, float a, float b, u32 ta, u32 tb) {
  u32x4 g = {__float_as_uint(a), ta, __float_as_uint(b), tb};
  __builtin_amdgcn_raw_buffer_store_b128(g, r, v, 0, AUX_SC1);
}
__device__ __forceinline__ u32 bad(u32x4 g, u32 ta, u32 tb) { return (g.y ^ ta) | (g.w ^ tb); }
__device__ __forceinline__ bool none(u32 b) { return __builtin_amdgcn_ballot_w64(b != 0) == 0; }
template <int CTRL> __device__ __forceinline__ float dpp(float x) {
  return __builtin_bit_cast(float, __builtin_amdgcn_update_dpp(0, __builtin_bit_cast(int, x), CTRL, 0xf, 0xf, true));
}
__device__ __forceinline__ float rsum16(float x) {
  x += dpp<0x128>(x); x += dpp<0x124>(x); x += dpp<0x122>(x); x += dpp<0x121>(x); return x;
}
__device__ __forceinline__ float xsum16(float x) {
  auto r = __builtin_amdgcn_permlane16_swap(__float_as_uint(x), __float_as_uint(x), false, false);
  return __uint_as_float(r[0]) + __uint_as_float(r[1]);
}
__device__ __forceinline__ float xsum32(float x) {
  auto r = __builtin_amdgcn_permlane32_swap(__float_as_uint(x), __float_as_uint(x), false, false);
  return __uint_as_float(r[0]) + __uint_as_float(r[1]);
}
__device__ __forceinline__ float xmax16(float x) {
  auto r = __builtin_amdgcn_permlane16_swap(__float_as_uint(x), __float_as_uint(x), false, false);
  return fmaxf(__uint_as_float(r[0]), __uint_as_float(r[1]));
}
__device__ __forceinline__ float xmax32(float x) {
  auto r = __builtin_amdgcn_permlane32_swap(__float_as_uint(x), __float_as_uint(x), false, false);
  return fmaxf(__uint_as_float(r[0]), __uint_as_float(r[1]));
}

__device__ __forceinline__ float sum8(float x) { x += dpp<0xB1>(x); x += dpp<0x4E>(x); x += dpp<0x141>(x); return x; }
__device__ __forceinline__ float max8(float x) {
  float r;
  asm("s_nop 1\n v_max_f32_dpp %0, %1, %1 quad_perm:[1,0,3,2] row_mask:0xf bank_mask:0xf\n"
      "s_nop 1\n v_max_f32_dpp %0, %0, %0 quad_perm:[2,3,0,1] row_mask:0xf bank_mask:0xf\n"
      "s_nop 1\n v_max_f32_dpp %0, %0, %0 row_half_mirror row_mask:0xf bank_mask:0xf" : "=&v"(r) : "v"(x));
  return r;
}
__device__ __forceinline__ u32 pk2(float a, float b) {
  bf16x2 r = {(__bf16)a, (__bf16)b};
  return __builtin_bit_cast(u32, r);
}
__device__ __forceinline__ float lo16(u32 x) { return __uint_as_float(x << 16); }
__device__ __forceinline__ float hi16(u32 x) { return __uint_as_float(x & 0xffff0000u); }
__device__ __forceinline__ float bfr(float x) { return hi16(pk2(x, x)); }
__device__ __forceinline__ float dot2(u32 a, u32 b, float c) {
  return __builtin_amdgcn_fdot2_f32_bf16(__builtin_bit_cast(bf16x2, a), __builtin_bit_cast(bf16x2, b), c, false);
}
__device__ __forceinline__ f32x4 mfma(u32x4 a, u32x4 b, f32x4 acc) {
  return __builtin_amdgcn_mfma_f32_16x16x32_bf16(__builtin_bit_cast(bf16x8, a), __builtin_bit_cast(bf16x8, b), acc, 0, 0, 0);
}
__device__ __forceinline__ f32x4 mma(u32x4 a, float v, const u32 (&mk4)[4], f32x4 acc) {
  const u32 bits2 = pk2(v, v);
  u32x4 b = {bits2 & mk4[0], bits2 & mk4[1], bits2 & mk4[2], bits2 & mk4[3]};
  return mfma(a, b, acc);
}
#define BAR() do { asm volatile("s_waitcnt lgkmcnt(0)" ::: "memory"); __builtin_amdgcn_s_barrier(); MEMFENCE(); SB(); } while (0)

#define STAMP(i)
#define STAMP_OUT()

__global__ void __launch_bounds__(512, 2)
fused_decode_kernel(const unsigned short* __restrict__ x, const unsigned short* __restrict__ kc,
                    const unsigned short* __restrict__ vc, const unsigned short* __restrict__ wq,
                    const unsigned short* __restrict__ wo, const unsigned short* __restrict__ w1,
                    const unsigned short* __restrict__ b1, const unsigned short* __restrict__ w2,
                    const unsigned short* __restrict__ b2, unsigned short* __restrict__ out, char* ws) {
  extern __shared__ __attribute__((aligned(16))) char lds[];
  const int lane = threadIdx.x & 63;
  const int w = __builtin_amdgcn_readfirstlane(threadIdx.x >> 6);
  const int blk = blockIdx.x;
  const int h = (blk >> 6) * 8 + (blk & 7), s = (blk >> 3) & 7;
  const int n = lane & 15, g = lane >> 4;
  const bool mlp = h < FF / 64;
  STAMP(0);
  asm volatile("" :: "s"(x), "s"(kc), "s"(vc), "s"(wq), "s"(wo), "s"(w1), "s"(b1), "s"(w2), "s"(b2), "s"(out), "s"(ws));

  const rsrc_t rx = mk(x, D * 2);
  u32x4 xa = ld16(rx, lane * 16, 0, 0);
  u32x4 xb = ld16(rx, 1024 + lane * 16, 0, 0);
  SB();
  const rsrc_t rwq = mk(wq + (size_t)(80 * s + 10 * w) * 4096 + h * 128, 10 * 8192);
  const rsrc_t rw1 = mk(w1 + (size_t)(80 * s + 10 * w) * FF + (mlp ? h : 0) * 64, mlp ? 9 * FF * 2 + 128 : 0);
  u32x4 aq[3], a1[2];
  aq[0] = ld16(rwq, g * 8192 + n * 16, 0, 1);
  aq[1] = ld16(rwq, g * 8192 + n * 16, 4 * 8192, 1);
  aq[2] = ld16(rwq, (8 + g) * 8192 + n * 16, 0, 1);
  a1[0] = ld16(rw1, (lane >> 3) * (FF * 2) + (lane & 7) * 16, 0, 1);
  a1[1] = ld16(rw1, (8 + (lane >> 3)) * (FF * 2) + (lane & 7) * 16, 0, 1);
  SB();
  __builtin_amdgcn_s_barrier();
  SB();
  const size_t kvo = ((size_t)h * SK + (SK - WIN) + 256 * s + 32 * w) * DH;
  const rsrc_t rk = mk(kc + kvo, 8192), rv = mk(vc + kvo, 8192);
  u32x4 kr[8], vr[8], wr[6];
  const u32 rot = ((w + s + h) & 7) * 1024;
  const u32 ksw = g * 256 + (n ^ (4 * g)) * 16;
#define LDK(i) kr[i] = ld16(rk, ksw ^ (((i) & 3) * 16), ((i) * 1024) ^ rot, 1)
#define LDV(i) vr[i] = ld16(rv, lane * 16, ((i) * 1024) ^ rot, 1)
#define STK(i) *(u32x4*)(lds + L_K + w * 8192 + (i) * 1024 + lane * 16) = kr[i]
  const int dbase = 32 * (w >> 1) + 4 * (w & 1);
  const int u0 = 32 * (w >> 2) + 2 * (w & 3) + (g >> 1) + 16 * (g & 1);
  const rsrc_t rwo = mk(wo + (size_t)(h * 128 + dbase) * D + 80 * s, 27 * 1280 + 160);
  const rsrc_t rw2 = mk(w2 + (size_t)((mlp ? h : 0) * 64) * D + 80 * s, mlp ? 63 * 1280 + 160 : 0);
  const u32 wooff = n < 10 ? g * 1280 + n * 16 : OOB, w2off = n < 10 ? u0 * 1280 + n * 16 : OOB;
#define LDW(j) do { if ((j) < 2) wr[(j) & 1] = ld16(rw2, w2off, ((j) & 1) * 10240, 1); \
    else wr[2 + (((j) - 2) & 3)] = ld16(rwo, wooff, (((j) - 2) & 3) * 10240, 1); } while (0)
#define ISSUE(i) do { SB(); if ((i) < 8) LDK((i) & 7); else if ((i) < 16) LDV((i) & 7); else if ((i) < 22) LDW((i) & 7); SB(); } while (0)
#pragma unroll
  for (int i = 0; i < WA; ++i) ISSUE(i);
  u64 ep;
  asm volatile("s_load_dwordx2 %0, %1, 0x0" : "=s"(ep) : "s"(ws) : "memory");
  SB();

  u32 mk4[4];
#pragma unroll
  for (int j = 0; j < 4; ++j) mk4[j] = (((n & 7) >> 1) == j) ? ((n & 1) ? 0xffff0000u : 0x0000ffffu) : 0u;

  char* lx = lds + L_X + w * 2048;
  *(u32x4*)(lx + lane * 16) = xa;
  *(u32x4*)(lx + 1024 + lane * 16) = xb;
  float sm = 0.f, sq = 0.f, sq2 = 0.f;
#pragma unroll
  for (int j = 0; j < 4; ++j) { sm = dot2(xa[j], 0x3f803f80u, sm); sm = dot2(xb[j], 0x3f803f80u, sm); }
  const float mean = xsum32(xsum16(rsum16(sm))) * (1.0f / D);
#pragma unroll
  for (int j = 0; j < 4; ++j) {
    const float d0 = lo16(xa[j]) - mean, d1 = hi16(xa[j]) - mean, d2 = lo16(xb[j]) - mean, d3 = hi16(xb[j]) - mean;
    sq = __builtin_fmaf(d0, d0, __builtin_fmaf(d1, d1, sq));
    sq2 = __builtin_fmaf(d2, d2, __builtin_fmaf(d3, d3, sq2));
  }
  const float var = xsum32(xsum16(rsum16(sq + (lane < 16 ? sq2 : 0.f)))) * (1.0f / D);
  const float mu = bfr(mean), rden = 1.0f / bfr(sqrtf(bfr(bfr(var) + 1e-5f)));
#define XN(r) (bfr(lo16(*(const unsigned short*)(lx + 2 * (r))) - mu) * rden)
  const int xr = 80 * s + 10 * w;
  f32x4 accq = {0.f, 0.f, 0.f, 0.f}, accp = {0.f, 0.f, 0.f, 0.f};
  accq = mma(aq[0], XN(xr + g), mk4, accq);
  accq = mma(aq[1], XN(xr + 4 + g), mk4, accq);
  accq = mma(aq[2], XN(xr + 8 + g), mk4, accq);
  accp = mma(a1[0], XN(xr + 2 * g + (n >> 3)), mk4, accp);
  accp = mma(a1[1], XN(xr + 8 + 2 * g + (n >> 3)), mk4, accp);
  STAMP(1);
  *(f32x4*)(lds + L_QP + w * 512 + (g * 8 + (n & 7)) * 16) = accq;
  *(f32x4*)(lds + (((g >> 1) == (n >> 3)) ? L_PP + (2 * w + (g >> 1)) * 256 + ((g & 1) * 8 + (n & 7)) * 16 : L_DUMMY)) = accp;
#pragma unroll
  for (int i = WA; i < WN; ++i) ISSUE(i);
  asm volatile("s_waitcnt lgkmcnt(0)" : "+s"(ep) :: "memory");
  u64 z = (ep + 0x632BE59BD9B4E019ull) * 0x9E3779B97F4A7C15ull;
  z = (z ^ (z >> 30)) * 0xBF58476D1CE4E5B9ull; z = (z ^ (z >> 27)) * 0x94D049BB133111EBull; z ^= z >> 31;
  const u32 ta = (u32)z, tb = (u32)(z >> 32);
  const rsrc_t rws = mk(ws, WS_END);
  const u32 hs = h * 8 + s;
  BAR();
  if (w == 0) {
    f32x2 a = {0.f, 0.f};
#pragma unroll
    for (int i = 0; i < 8; ++i) a += *(const f32x2*)(lds + L_QP + i * 512 + lane * 8);
    stg(rws, WS_QG + hs * 1024 + lane * 16, a.x, a.y, ta, tb);
  } else if (w == 1) {
    f32x2 a = {0.f, 0.f};
#pragma unroll
    for (int i = 0; i < 8; ++i) a += *(const f32x2*)(lds + L_PP + i * 256 + (lane >> 5) * 2048 + (lane & 31) * 8);
    a.x = xsum32(a.x); a.y = xsum32(a.y);
    stg(rws, lane < 32 ? WS_PG + hs * 512 + lane * 16 : OOB, a.x, a.y, ta, tb);
  }
  MEMFENCE(); SB();
  STAMP(2);
#pragma unroll
  for (int i = 0; i <= PP; ++i) { STK(i); ISSUE(i + WN); }
  const u32 hp = h * 8 + (n & 7);
  const u32 gsel = (w * 8 + 2 * g + (n >> 3)) * 16;
  const u32 qoff = WS_QG + hp * 1024 + gsel;
  const u32 poff = WS_PG + hp * 512 + (w * 4 + g) * 16;
  u32x4 qg = ldg(rws, qoff);
  u32x4 pg = ldg(rws, poff);
  const rsrc_t rb1 = mk(b1 + (mlp ? h : 0) * 64, 128);
  const u32 b1a = __builtin_amdgcn_raw_buffer_load_b16(rb1, u0 * 2, 0, 0);
  const u32 b1b = __builtin_amdgcn_raw_buffer_load_b16(rb1, u0 * 2 + 16, 0, 0);
  const int ft = (blk % 5) * 8 + (lane >> 3);
  const int fc = 80 * (blk / 5) + (ft < 32 ? 32 * (ft >> 4) + 16 * (ft & 1) + ((ft >> 1) & 7) : 32 + ft);
  const rsrc_t rfx = mk(x, w == 5 ? D * 2 : 0), rb2 = mk(b2, w == 5 ? D * 2 : 0);
  const u32 fx0 = __builtin_amdgcn_raw_buffer_load_b16(rfx, fc * 2, 0, 0), fx1 = __builtin_amdgcn_raw_buffer_load_b16(rfx, fc * 2 + 16, 0, 0);
  const u32 fb0 = __builtin_amdgcn_raw_buffer_load_b16(rb2, fc * 2, 0, 0), fb1 = __builtin_amdgcn_raw_buffer_load_b16(rb2, fc * 2 + 16, 0, 0);
  SB();
#pragma unroll
  for (int i = PP + 1; i < 8; ++i) { STK(i); ISSUE(i + WN); }
  constexpr int NP = PP + WN - 8;
#pragma unroll
  for (int i = 0; i < NP; ++i) { asm volatile("" : "+v"(vr[i])); ISSUE(8 + i + WN); }
  if (!(none(bad(qg, ta, tb) | bad(pg, ta, tb)))) {
    _Pragma("unroll 1") for (int it = 0; it < SPIN_CAP; ++it) {
      MEMFENCE();
      qg = ldg(rws, qoff);
      pg = ldg(rws, poff);
      if (none(bad(qg, ta, tb) | bad(pg, ta, tb))) break;
    }
  }
  MEMFENCE();
  STAMP(3);
  constexpr int NX = 8 + NP + WN;
  ISSUE(NX);
  {
    const u32 qq = pk2(sum8(__uint_as_float(qg.x)), sum8(__uint_as_float(qg.z)));
    unsigned short* lq = (unsigned short*)(lds + L_Q) + dbase + g + 16 * (n >> 3);
    lq[0] = (unsigned short)qq;
    lq[8] = (unsigned short)(qq >> 16);
  }
#pragma unroll
  for (int j = 0; j < 8; ++j)
    kr[j] = *(const u32x4*)(lds + L_K + w * 8192 + (j >> 2) * 4096 + (n & 3) * 1024 + (n >> 2) * 256 + (((g ^ n) * 16) ^ ((j & 3) * 64)));
  BAR();
  f32x4 s0 = {0.f, 0.f, 0.f, 0.f}, s1 = {0.f, 0.f, 0.f, 0.f};
#pragma unroll
  for (int j = 0; j < 4; ++j) {
    const u32x4 q = *(const u32x4*)(lds + L_Q + j * 64 + g * 16);
    s0 = mfma(kr[j], q, s0);
    s1 = mfma(kr[4 + j], q, s1);
  }
  ISSUE(NX + 1);
  float sc[8];
#pragma unroll
  for (int i = 0; i < 4; ++i) {
    const u32 a = pk2(s0[i], s1[i]);
    const u32 b = pk2(lo16(a) * 0.08838834764831845f, hi16(a) * 0.08838834764831845f);
    sc[i] = lo16(b); sc[4 + i] = hi16(b);
  }
  float m = fmaxf(fmaxf(fmaxf(sc[0], sc[1]), fmaxf(sc[2], sc[3])), fmaxf(fmaxf(sc[4], sc[5]), fmaxf(sc[6], sc[7])));
  m = xmax32(xmax16(m));
  *(float*)(lds + L_M + w * 4) = m;
  BAR();
  ISSUE(NX + 2);
  m = max8(*(const float*)(lds + L_M + (lane & 7) * 4));
  float l = 0.f;
  const float LOG2E = 1.4426950408889634f;
#pragma unroll
  for (int i = 0; i < 8; ++i) { sc[i] = __builtin_amdgcn_exp2f((sc[i] - m) * LOG2E); l += sc[i]; }
  l = xsum32(xsum16(l));
  *(float*)(lds + L_L + w * 4) = l;
  float hv0 = sum8(__uint_as_float(pg.x)), hv1 = sum8(__uint_as_float(pg.z));
  {
    u32 a = pk2(hv0, hv1);
    a = pk2(lo16(a) + lo16(b1a), hi16(a) + lo16(b1b));
    hv0 = lo16(a); hv1 = hi16(a);
    const float c2 = -2.0f * 0.7978845608028654f * LOG2E;
    hv0 *= __builtin_amdgcn_rcpf(1.0f + __builtin_amdgcn_exp2f(c2 * (hv0 + 0.044715f * hv0 * hv0 * hv0)));
    hv1 *= __builtin_amdgcn_rcpf(1.0f + __builtin_amdgcn_exp2f(c2 * (hv1 + 0.044715f * hv1 * hv1 * hv1)));
  }
  STAMP(4);
  u32 a_opr = L_OP + (lane & 7) * 512 + (4 * w + g) * 16, a_lr = L_L + (lane & 7) * 4;
  u32 a_ost = (n == 0 || lane == 8) ? WS_OG + hs * 1024 + w * 128 + (n < 8 ? g * 16 : 64) : OOB;
  u32 ooff = (n & 7) == s ? OOB : WS_OG + hp * 1024 + w * 128 + (n < 8 ? g * 16 : 64);
  u32 a_ypw = n < 8 ? L_YP + w * 512 + (g * 8 + n) * 16 : L_DUMMY;
  u32 a_ypr = L_YP + (lane & 7) * 512 + (w < 4 ? w * 64 + (lane >> 3) * 8 : 256 + (lane >> 3) * 16);
  u32 a_y = w < 5 ? ((lane & 7) == 0 ? WS_YG + (5 * s + w) * 4096 + h * 128 + lane * 2 : OOB)
                  : WS_YG + blk * 4096 + (lane & 7) * 128 + (lane >> 3) * 16;
  float fr0 = lo16(fx0) + lo16(fb0), fr1 = lo16(fx1) + lo16(fb1);
  asm volatile("" : "+v"(a_opr), "+v"(a_lr), "+v"(a_ost), "+v"(ooff), "+v"(a_ypw), "+v"(a_ypr), "+v"(a_y), "+v"(fr0),
               "+v"(fr1), "+v"(hv0), "+v"(hv1));
  f32x4 acco = {0.f, 0.f, 0.f, 0.f};
#pragma unroll
  for (int i = 0; i < 8; ++i) {
    acco = mma(vr[i], sc[i], mk4, acco);
    ISSUE(NX + 3 + i);
  }
  *(f32x4*)(lds + L_OP + w * 512 + (g * 8 + (n & 7)) * 16) = acco;
  STAMP(5);
  BAR();
  u32 r0, r1;
  {
    const f32x4 a = *(const f32x4*)(lds + a_opr);
    const float L = sum8(*(const float*)(lds + a_lr));
    const u32 p01 = pk2(sum8(a[0]), sum8(a[1])), p23 = pk2(sum8(a[2]), sum8(a[3]));
    r0 = n < 8 ? p01 : __float_as_uint(m);
    r1 = n < 8 ? p23 : __float_as_uint(L);
    stg(rws, a_ost, __uint_as_float(r0), __uint_as_float(r1), ta, tb);
  }
  MEMFENCE(); SB();
  STAMP(10);
  f32x4 accy = {0.f, 0.f, 0.f, 0.f};
  {
    u64 done = __builtin_amdgcn_ballot_w64((n & 7) == s), tmp;
    int cnt = SPIN_CAP;
    __builtin_amdgcn_s_sleep(DLY);
    asm volatile("buffer_load_dwordx4 v[200:203], %[o], %[rs], 0 offen sc1" :: [o] "v"(ooff), [rs] "s"(rws)
                 : "memory", "v200", "v201", "v202", "v203");
    accy = mma(wr[0], hv0, mk4, accy);
    accy = mma(wr[1], hv1, mk4, accy);
    asm volatile("" : "+v"(accy), "+v"(wr[2]), "+v"(wr[3]), "+v"(wr[4]), "+v"(wr[5]));
#define LATCH(X, A, Z, B) \
    "v_cmp_eq_u32 vcc, %[ta], " A "\n v_cmp_eq_u32 %[tmp], %[tb], " B "\n s_and_b64 vcc, vcc, %[tmp]\n s_andn2_b64 vcc, vcc, %[done]\n" \
    "s_or_b64 %[done], %[done], vcc\n v_cndmask_b32 %[r0], %[r0], " X ", vcc\n v_cndmask_b32 %[r1], %[r1], " Z ", vcc\n" \
    "v_cndmask_b32 %[o], %[o], %[oob], vcc\n s_cmp_eq_u64 %[done], -1\n s_cbranch_scc1 2f\n"
    asm volatile(
      "s_waitcnt vmcnt(0)\n" LATCH("v200", "v201", "v202", "v203")
      "buffer_load_dwordx4 v[200:203], %[o], %[rs], 0 offen sc1\n"
      "1:\n s_sleep %[sl]\n"
      "buffer_load_dwordx4 v[204:207], %[o], %[rs], 0 offen sc1\n"
      "s_waitcnt vmcnt(1)\n" LATCH("v200", "v201", "v202", "v203")
      "s_sleep %[sl]\n"
      "buffer_load_dwordx4 v[200:203], %[o], %[rs], 0 offen sc1\n"
      "s_waitcnt vmcnt(1)\n" LATCH("v204", "v205", "v206", "v207")
      "s_sub_u32 %[cnt], %[cnt], 1\n s_cmp_lg_u32 %[cnt], 0\n s_cbranch_scc1 1b\n"
      "2:\n s_nop 1\n"
      : [r0] "+v"(r0), [r1] "+v"(r1), [o] "+v"(ooff), [done] "+s"(done), [tmp] "=&s"(tmp), [cnt] "+s"(cnt)
      : [rs] "s"(rws), [ta] "s"(ta), [tb] "s"(tb), [oob] "v"(OOB), [sl] "n"(PIPE)
      : "memory", "vcc", "scc", "v200", "v201", "v202", "v203", "v204", "v205", "v206", "v207");
  }
  STAMP(6);
  {
    const float ms = __uint_as_float(r0);
    const float e = __builtin_amdgcn_exp2f((ms - max8(ms)) * LOG2E);
    const float eo = dpp<0x128>(e);
    float t[5] = {lo16(r0) * eo, hi16(r0) * eo, lo16(r1) * eo, hi16(r1) * eo, __uint_as_float(r1) * e};
    SB();
#pragma unroll
    for (int i = 0; i < 5; ++i) t[i] += dpp<0xB1>(t[i]);
    SB();
#pragma unroll
    for (int i = 0; i < 5; ++i) t[i] += dpp<0x4E>(t[i]);
    SB();
#pragma unroll
    for (int i = 4; i >= 0; --i) t[i] += dpp<0x141>(t[i]);
    SB();
    const float rl = dpp<0x128>(__builtin_amdgcn_rcpf(t[4]));
    f32x4 acc2 = {0.f, 0.f, 0.f, 0.f};
    accy = mma(wr[2], t[0] * rl, mk4, accy);
    acc2 = mma(wr[3], t[1] * rl, mk4, acc2);
    accy = mma(wr[4], t[2] * rl, mk4, accy);
    acc2 = mma(wr[5], t[3] * rl, mk4, acc2);
    accy += acc2;
    *(f32x4*)(lds + a_ypw) = accy;
  }
  BAR();
  STAMP(7);
  if (w < 5) {
    const f32x2 a = *(const f32x2*)(lds + a_ypr);
    stg(rws, a_y, sum8(a.x), sum8(a.y), ta, tb);
    STAMP(8);
  } else if (w == 5 && blk < 40) {
    float y0, y1, t0, t1;
    int cnt = SPIN_CAP;

#define YL4(A, B, C, E) \
    "buffer_load_dwordx4 " A ", %[o], %[rs], 0 offen sc1\n buffer_load_dwordx4 " B ", %[o], %[rs], 0 offen offset:1024 sc1\n" \
    "buffer_load_dwordx4 " C ", %[o], %[rs], 0 offen offset:2048 sc1\n buffer_load_dwordx4 " E ", %[o], %[rs], 0 offen offset:3072 sc1\n"
#define CHK4(A, B, C, E, F, G, H, I) \
    "v_xor_b32 %[r0], %[ta], " A "\n v_xor_b32 %[r1], %[tb], " B "\n v_xor_b32 %[r2], %[ta], " C "\n v_xor_b32 %[r3], %[tb], " E "\n" \
    "v_or3_b32 %[r0], %[r0], %[r1], %[r2]\n v_xor_b32 %[r1], %[ta], " F "\n v_xor_b32 %[r2], %[tb], " G "\n v_or3_b32 %[r0], %[r0], %[r1], %[r2]\n" \
    "v_xor_b32 %[r1], %[ta], " H "\n v_xor_b32 %[r2], %[tb], " I "\n v_or3_b32 %[r1], %[r3], %[r1], %[r2]\n" \
    "v_or_b32 %[r0], %[r0], %[r1]\n v_cmp_ne_u32 vcc, 0, %[r0]\n"
    asm volatile(
      YL4("v[216:219]", "v[220:223]", "v[224:227]", "v[228:231]")
      "1:\n s_sleep %[sl]\n" YL4("v[232:235]", "v[236:239]", "v[240:243]", "v[244:247]")
      "s_waitcnt vmcnt(4)\n" CHK4("v217", "v219", "v221", "v223", "v225", "v227", "v229", "v231")
      "s_cbranch_vccz 2f\n s_sleep %[sl]\n" YL4("v[216:219]", "v[220:223]", "v[224:227]", "v[228:231]")
      "s_waitcnt vmcnt(4)\n" CHK4("v233", "v235", "v237", "v239", "v241", "v243", "v245", "v247")
      "s_cbranch_vccz 3f\n s_sub_u32 %[cnt], %[cnt], 1\n s_cmp_lg_u32 %[cnt], 0\n s_cbranch_scc1 1b\n"
      "3:\n v_add_f32 %[r0], v232, v236\n v_add_f32 %[r1], v234, v238\n v_add_f32 %[r2], v240, v244\n v_add_f32 %[r3], v242, v246\n s_branch 4f\n"
      "2:\n v_add_f32 %[r0], v216, v220\n v_add_f32 %[r1], v218, v222\n v_add_f32 %[r2], v224, v228\n v_add_f32 %[r3], v226, v230\n"
      "4:\n"
      : [r0] "=&v"(y0), [r1] "=&v"(y1), [r2] "=&v"(t0), [r3] "=&v"(t1), [cnt] "+s"(cnt)
      : [o] "v"(a_y), [rs] "s"(rws), [ta] "s"(ta), [tb] "s"(tb), [sl] "n"(PIPE2)
      : "memory", "vcc", "scc", "v216", "v217", "v218", "v219", "v220", "v221", "v222", "v223", "v224", "v225", "v226",
        "v227", "v228", "v229", "v230", "v231", "v232", "v233", "v234", "v235", "v236", "v237", "v238", "v239", "v240",
        "v241", "v242", "v243", "v244", "v245", "v246", "v247");
    y0 += t0; y1 += t1;
    const u32 r = pk2(sum8(y0) + fr0, sum8(y1) + fr1);
    if ((lane & 7) == 0) { out[fc] = (unsigned short)r; out[fc + 8] = (unsigned short)(r >> 16); }
    if (blk == 0 && lane == 0) __hip_atomic_store((u64*)ws, ep + 1, __ATOMIC_RELAXED, __HIP_MEMORY_SCOPE_AGENT);
    STAMP(9);
  }
  STAMP_OUT();
  asm volatile("s_waitcnt vmcnt(0)" ::: "memory");
}

extern "C" void kernel_launch(void* const* d_in, const int* in_sizes, int n_in, void* d_out, int out_size,
                              void* d_ws, size_t ws_size, hipStream_t stream) {
  typedef const unsigned short* P;
  (void)hipFuncSetAttribute((const void*)fused_decode_kernel, hipFuncAttributeMaxDynamicSharedMemorySize, LDS_BYTES);
  fused_decode_kernel<<<256, 512, LDS_BYTES, stream>>>((P)d_in[0], (P)d_in[1], (P)d_in[2], (P)d_in[3], (P)d_in[4],
                                                       (P)d_in[6], (P)d_in[7], (P)d_in[8], (P)d_in[9],
                                                       (unsigned short*)d_out, (char*)d_ws);
}
```
